```python
import torch

def _softmax(x, axis=-1):
    return torch.softmax(x, dim=axis)
def _gelu(x):
    return torch.nn.functional.gelu(x, approximate="tanh")
def _silu(x):
    return torch.nn.functional.silu(x)
def _sigmoid(x):
    return torch.sigmoid(x)
def _layernorm(x, axis=-1, eps=1e-5):
    m = x.mean(dim=axis, keepdim=True)
    v = x.var(dim=axis, keepdim=True, unbiased=False)
    return (x - m) / torch.sqrt(v + eps)
def _scatter_add(tgt, idx, src, axis=0):
    return tgt.clone().index_add_(axis, idx, src)
def _segment_sum(src, seg, n):
    out = torch.zeros((n,) + src.shape[1:], dtype=src.dtype)
    return out.index_add_(0, seg, src)
def _pad(x, pw):
    flat = tuple(v for pair in reversed(pw) for v in pair)
    return torch.nn.functional.pad(x, flat)

kernel_name = 'parallel_sum__sliding_local_dilated__glu_gated_reglu__prompt'

def setup_inputs():

    _w = (lambda a: (lambda t: t.to(torch.bfloat16) if t.is_floating_point() else t)(torch.as_tensor(a)))
    def _A_setup():
        B, S, D = 1, 2048, 576
        return {
            "x": _w((lambda *s: torch.randn(*s, dtype=torch.bfloat16) * 0.05)(B, S, D)),
            "wqkv": _w((lambda *s: torch.randn(*s, dtype=torch.bfloat16) * 0.05)(D, 3 * D)),
            "wo": _w((lambda *s: torch.randn(*s, dtype=torch.bfloat16) * 0.05)(D, D)),
        }
    _A_in = _A_setup()

    def _B_setup():
        B, S, D, F = 1, 2048, 576, 4608
        return {
            "x": _w((lambda *s: torch.randn(*s, dtype=torch.bfloat16) * 0.05)(B, S, D)),
            "W_gate": _w((lambda *s: torch.randn(*s, dtype=torch.bfloat16) * 0.8)(D, F) * (2.0 / D) ** 0.5),
            "W_up": _w((lambda *s: torch.randn(*s, dtype=torch.bfloat16) * 0.8)(D, F) * (2.0 / D) ** 0.5),
            "W_down": _w((lambda *s: torch.randn(*s, dtype=torch.bfloat16) * 0.8)(F, D) * (1.0 / F) ** 0.5),
        }
    _B_in = _B_setup()

    return {
        "A_x": _A_in["x"],
        "A_wqkv": _A_in["wqkv"],
        "A_wo": _A_in["wo"],
        "B_x": _B_in["x"],
        "B_W_gate": _B_in["W_gate"],
        "B_W_up": _B_in["W_up"],
        "B_W_down": _B_in["W_down"],
    }

def reference(A_x, A_wqkv, A_wo, B_x, B_W_gate, B_W_up, B_W_down):
    def _A_ref(x, wqkv, wo):
        B, S, D = x.shape
        H = 32; Dh = D // H
        W, dil = 8, 2
        qkv = (lambda x, *d: x.permute(*d))((x @ wqkv).reshape(B, S, 3, H, Dh), 2, 0, 3, 1, 4)
        q, k, v = qkv[0], qkv[1], qkv[2]
        scale = Dh ** -0.5
        pos = torch.arange(S)
        off = torch.arange(W) * dil
        idx = pos[:, None] - off[None, :]
        mask = idx >= 0
        idx_c = torch.where(mask, idx, 0).reshape(-1)
        kg = k[:, :, idx_c, :].reshape(B, H, S, W, Dh)
        vg = v[:, :, idx_c, :].reshape(B, H, S, W, Dh)
        logits = torch.einsum("bhsd,bhswd->bhsw", q, kg) * scale
        logits = torch.where(mask[None, None, :, :], logits, -1e9)
        attn = _softmax(logits, axis=-1)
        o = torch.einsum("bhsw,bhswd->bhsd", attn, vg)
        return (lambda x, *d: x.permute(*d))(o, 0, 2, 1, 3).reshape(B, S, D) @ wo

    def _B_ref(x, W_gate, W_up, W_down):
        gate = torch.einsum("bsd,df->bsf", x, W_gate)
        up = torch.einsum("bsd,df->bsf", x, W_up)
        gate = gate * (lambda x, dt: x.to(dt))(gate > 0, gate.dtype)
        h = gate * up
        return torch.einsum("bsf,fd->bsd", h, W_down)

    x = A_x
    xn = _layernorm(x, axis=-1)
    return x + _A_ref(xn, A_wqkv, A_wo) + _B_ref(xn, B_W_gate, B_W_up, B_W_down)
```

```cpp
#include <hip/hip_runtime.h>
#include <hip/hip_bf16.h>
#include <stdint.h>
#include <type_traits>

using bf16x8 = __attribute__((ext_vector_type(8))) short;
using s16x4  = __attribute__((ext_vector_type(4))) short;
using f32x16 = __attribute__((ext_vector_type(16))) float;
using f32x2  = __attribute__((ext_vector_type(2))) float;
using u32x4  = __attribute__((ext_vector_type(4))) unsigned;
using u32x2  = __attribute__((ext_vector_type(2))) unsigned;
#define KT(i)
#define LAS __attribute__((address_space(3)))
typedef LAS const char* lds_cptr;
typedef LAS char* lds_ptr;
typedef short v4i16_t __attribute__((ext_vector_type(4)));
typedef __bf16 bf16x2_t __attribute__((ext_vector_type(2)));
#define DI __device__ __forceinline__
#define SBAR() __builtin_amdgcn_sched_barrier(0)

constexpr int S_ = 2048, D_ = 576, F_ = 4608;
constexpr int NKS = 36;
constexpr int NCH = 16;
constexpr int HSLOT = 22528;
constexpr int NHS = 7;
constexpr int LDS_BYTES = 163840;

DI float b2f(unsigned short b) { return __uint_as_float((unsigned)b << 16); }
DI unsigned cvtpk(float lo, float hi) { f32x2 v = {lo, hi}; bf16x2_t b = __builtin_convertvector(v, bf16x2_t); return __builtin_bit_cast(unsigned, b); }
DI float rbf(float f) { return __uint_as_float(cvtpk(f, 0.f) << 16); }
DI float lo_f(unsigned p) { return __uint_as_float(p << 16); }
DI float hi_f(unsigned p) { return __uint_as_float(p & 0xffff0000u); }
DI s16x4 vtr(lds_cptr p) { return __builtin_bit_cast(s16x4, __builtin_amdgcn_ds_read_tr16_b64_v4i16((LAS v4i16_t*)p)); }
DI bf16x8 trfrag(lds_cptr p, int o0, int o1) { s16x4 a = vtr(p + o0); SBAR(); s16x4 b = vtr(p + o1); return __builtin_shufflevector(a, b, 0, 1, 2, 3, 4, 5, 6, 7); }
DI void glds16s(unsigned voff, const void* sbase, unsigned lds_base) {
  asm volatile("s_mov_b32 m0, %2\n\ts_nop 0\n\tglobal_load_lds_dwordx4 %0, %1" :: "v"(voff), "s"(sbase), "s"(lds_base) : "memory", "m0");
}
#define PSTORE(p, v) __builtin_nontemporal_store((v), (u32x4*)(p))
#define MFMA(a, b, c) __builtin_amdgcn_mfma_f32_32x32x16_bf16((a), (b), (c), 0, 0, 0)
template <int N> DI void wait_bar() { asm volatile("s_waitcnt vmcnt(%0)\n\ts_barrier" :: "i"(N) : "memory"); }
template <int N> DI void wait_bar_l() { asm volatile("s_waitcnt vmcnt(%0) lgkmcnt(0)\n\ts_barrier" :: "i"(N) : "memory"); }

#define STAMP(i)
#define STAMP1(i)

constexpr int K1_XH = 2 * NKS * 1024;
constexpr int K1_XS = K1_XH + NKS * 512;
constexpr int K1_WP = 448;
constexpr int K1_WST = 32 * K1_WP;
constexpr int K1_NW = 5;
constexpr int K1_QP = 488;
constexpr int K1_LDS = K1_XS + K1_NW * K1_WST;
DI float dot2(unsigned a, unsigned b, float c) { return __builtin_amdgcn_fdot2_f32_bf16(__builtin_bit_cast(bf16x2_t, a), __builtin_bit_cast(bf16x2_t, b), c, false); }
__global__ __launch_bounds__(512, 2) void k_attn(const unsigned short* __restrict__ x, const char* __restrict__ wqkv, char* __restrict__ xf, char* __restrict__ of) {
  extern __shared__ __attribute__((aligned(16))) char lds[];
  STAMP1(0);
  const int tid = threadIdx.x, lane = tid & 63;
  const unsigned wid = __builtin_amdgcn_readfirstlane(tid >> 6);
  const unsigned hg = blockIdx.x & 7, mt = blockIdx.x >> 3;
  const int t0 = 64 * mt;
  const unsigned l0 = (unsigned)(uintptr_t)lds;
  const lds_cptr ldsb = (lds_cptr)lds;
  unsigned wv[2];
#pragma unroll
  for (int i = 0; i < 2; ++i) { const unsigned g = (wid + 8 * i) * 64 + lane, row = g / 28; unsigned ch = g - row * 28; if (ch > 26) ch = 26;
    const unsigned part = ch / 9, cc = ch - part * 9; const unsigned colb = (part == 0 ? 1152u : part == 1 ? 2304u : 0u) + 144 * hg + cc * 16;
    wv[i] = row * 3456 + colb; }
  auto wdma = [&](int st) { const char* b = wqkv + (size_t)st * 32 * 3456; const unsigned d = l0 + K1_XS + (st % K1_NW) * K1_WST;
    glds16s(wv[0], b, d + wid * 1024); if (wid < 6) glds16s(wv[1], b, d + (wid + 8) * 1024); };
  {
    const int tl = lane / 6, j = lane - tl * 6; const bool act = lane < 60;
    const int slot = wid * 10 + (act ? tl : 9); const int t = t0 - 16 + slot;
    const u32x4* src = (const u32x4*)(x + (size_t)(t >= 0 ? t : 0) * D_) + j;
    u32x4 v[12];
    KT(0);
#pragma unroll
    for (int i = 0; i < 12; ++i) v[i] = src[6 * i];
    KT(1);
    wdma(0); wdma(1); wdma(2); wdma(3);
    KT(2);
    float s = 0, q = 0;
#pragma unroll
    for (int i = 0; i < 12; ++i)
#pragma unroll
      for (int e = 0; e < 4; ++e) { s = dot2(v[i][e], 0x3f803f80u, s); q = dot2(v[i][e], v[i][e], q); }
    KT(3);
    const int lb = (act ? tl : 9) * 6;
    { const int j3 = j >= 3 ? j - 3 : j + 3, j1 = j + 1 >= 6 ? j - 5 : j + 1, j2 = j + 2 >= 6 ? j - 4 : j + 2;
      s += __shfl(s, lb + j3); q += __shfl(q, lb + j3);
      const float s1 = __shfl(s, lb + j1), s2 = __shfl(s, lb + j2), q1 = __shfl(q, lb + j1), q2 = __shfl(q, lb + j2);
      s += s1 + s2; q += q1 + q2; }
    const float m = s * (1.f / D_);
    const float var = fmaxf(q * (1.f / D_) - m * m, 0.f);
    const float mb = rbf(m), vb = rbf(var);
    const float ri = 1.f / rbf(sqrtf(rbf(vb + 1e-5f)));
    KT(4);
    const bool halo = slot < 16; const int ms = slot - 16, tb = ms >> 5, r = ms & 31;
    lds_ptr dst = halo ? (lds_ptr)lds + K1_XH + slot * 16 : (lds_ptr)lds + tb * (NKS * 1024) + r * 16;
    const int kstride = halo ? 512 : 1024, hstride = halo ? 256 : 512;
    char* gdst = xf + (size_t)((2 * mt + tb) * NKS) * 1024 + r * 16;
    const bool wr = act && !halo && ((ms >> 3) & 7) == (int)hg;
    const f32x2 mb2 = {mb, mb}, ri2 = {ri, ri};
#pragma unroll
    for (int i = 0; i < 12; ++i) {
      u32x4 o;
#pragma unroll
      for (int e = 0; e < 4; ++e) { f32x2 a = {lo_f(v[i][e]), hi_f(v[i][e])}; a -= mb2; const unsigned d = cvtpk(a[0], a[1]); f32x2 b = {lo_f(d), hi_f(d)}; b *= ri2; o[e] = cvtpk(b[0], b[1]); }
      const int qc = j + 6 * i;
      if (act) *(LAS u32x4*)(dst + (qc >> 1) * kstride + (qc & 1) * hstride) = o;
      if (wr) *(u32x4*)(gdst + (qc >> 1) * 1024 + (qc & 1) * 512) = o;
    }
  }
  KT(5);
  STAMP1(1);
  const unsigned h = lane >> 5, blk16 = (lane >> 4) & 1, q4 = (lane & 15) >> 2, p4 = lane & 3;
  const unsigned cbA = wid < 7 ? wid : 0;
  const unsigned rdbase = K1_XS + (8 * h + q4) * K1_WP + blk16 * 32 + p4 * 8 + cbA * 64;
  const bool three = wid >= 1 && wid <= 3;
  f32x16 a0 = {}, a1 = {}, a2 = {};
  const lds_cptr xm = ldsb + lane * 16, xh = ldsb + K1_XH + h * 256 + (lane & 15) * 16;
  auto gemm = [&](auto role_) {
    constexpr int ROLE = decltype(role_)::value;
    struct Fr { bf16x8 a[2], b[2], c[2], d[2]; };
    auto rdst = [&](Fr& f, int st) {
      const lds_cptr wb = ldsb + rdbase + (st % K1_NW) * K1_WST;
#pragma unroll
      for (int k2 = 0; k2 < 2; ++k2) {
        const int ks = st * 2 + k2;
        f.a[k2] = trfrag(wb, k2 * 16 * K1_WP, (k2 * 16 + 4) * K1_WP);
        if (ROLE < 2) { f.b[k2] = *(const LAS bf16x8*)(xm + ks * 1024); f.c[k2] = *(const LAS bf16x8*)(xm + (NKS + ks) * 1024); if (ROLE == 1) f.d[k2] = *(const LAS bf16x8*)(xh + ks * 512); }
        else { f.b[k2] = trfrag(wb + 256, k2 * 16 * K1_WP, (k2 * 16 + 4) * K1_WP); f.c[k2] = *(const LAS bf16x8*)(xh + ks * 512); }
      }
    };
    auto mm = [&](const Fr& f) {
#pragma unroll
      for (int k2 = 0; k2 < 2; ++k2) {
        if (ROLE < 2) { a1 = MFMA(f.a[k2], f.b[k2], a1); a2 = MFMA(f.a[k2], f.c[k2], a2); if (ROLE == 1) a0 = MFMA(f.a[k2], f.d[k2], a0); }
        else { a1 = MFMA(f.a[k2], f.c[k2], a1); a2 = MFMA(f.b[k2], f.c[k2], a2); }
      }
    };
    Fr fa, fb;
    wait_bar_l<0>();
    KT(6);
    rdst(fa, 0);
#pragma unroll 1
    for (int st = 0; st < 18; st += 2) {
      KT(7 + st);
      if (st + 1 + 2 < 18 && st >= 2) { if (wid < 6) wait_bar<4>(); else wait_bar<2>(); } else wait_bar<0>();
      KT(8 + st);
      asm volatile("" ::: "memory");
      if (st + 4 < 18) wdma(st + 4);
      rdst(fb, st + 1);
      mm(fa);
      asm volatile("s_waitcnt lgkmcnt(0)" ::: "memory");
      if (st + 2 < 18) {
        if (st + 2 + 2 < 18) { if (wid < 6) wait_bar<4>(); else wait_bar<2>(); } else wait_bar<0>();
        if (st + 5 < 18) wdma(st + 5);
        rdst(fa, st + 2);
      }
      mm(fb);
      asm volatile("s_waitcnt lgkmcnt(0)" ::: "memory");
    }
  };
  if (wid == 7) gemm(std::integral_constant<int, 2>{}); else if (three) gemm(std::integral_constant<int, 1>{}); else gemm(std::integral_constant<int, 0>{});
  KT(25);
  STAMP1(2);
  asm volatile("s_waitcnt lgkmcnt(0)\n\ts_barrier" ::: "memory");
  {
    auto put = [&](const f32x16& a, unsigned rowslot, unsigned cb, bool on) {
      lds_ptr d = (lds_ptr)lds + rowslot * K1_QP;
#pragma unroll
      for (int g = 0; g < 4; ++g)
#pragma unroll
        for (int e = 0; e < 2; ++e) {
          const unsigned c = cb * 32 + 8 * g + 4 * h + 2 * e, part = c / 72, rem = c - part * 72, head = rem / 18, dd = rem - head * 18;
          if (on && c < 216) *(LAS unsigned*)(d + part * 160 + head * 40 + dd * 2) = cvtpk(a[4 * g + 2 * e], a[4 * g + 2 * e + 1]);
        }
    };
    const unsigned r = lane & 31;
    if (wid < 7) { put(a1, 16 + r, wid, true); put(a2, 48 + r, wid, true); if (three) put(a0, r & 15, wid, r < 16); }
    else { put(a1, r & 15, 0, r < 16); put(a2, r & 15, 4, r < 16); }
  }
  asm volatile("s_waitcnt lgkmcnt(0)\n\ts_barrier" ::: "memory");
  KT(26);
  {
    const int tok = (tid & 31) + 32 * ((tid >> 6) & 1), part = (tid >> 5) & 1, head = tid >> 7;
    const int t = t0 + tok, slot = 16 + tok;
    const lds_cptr qp = ldsb + slot * K1_QP + 320 + head * 40;
    unsigned qv[10];
#pragma unroll
    for (int i = 0; i < 5; ++i) { const u32x2 u = *(const LAS u32x2*)(qp + 8 * i); qv[2 * i] = u[0]; qv[2 * i + 1] = u[1]; }
    float lg[4];
#pragma unroll
    for (int i4 = 0; i4 < 4; ++i4) {
      const int w = 4 * part + i4;
      const lds_cptr kp = ldsb + (slot - 2 * w) * K1_QP + head * 40;
      float a = 0;
#pragma unroll
      for (int i = 0; i < 5; ++i) { const u32x2 u = *(const LAS u32x2*)(kp + 8 * i); a = dot2(qv[2 * i], u[0], a); if (i < 4) a = dot2(qv[2 * i + 1], u[1], a); }
      lg[i4] = (t - 2 * w >= 0) ? rbf(rbf(a) * 0.23570226039551584f) : -1e9f;
    }
    float mx = fmaxf(fmaxf(lg[0], lg[1]), fmaxf(lg[2], lg[3]));
    mx = fmaxf(mx, __shfl_xor(mx, 32));
    float sum = 0;
#pragma unroll
    for (int i = 0; i < 4; ++i) { lg[i] = __expf(lg[i] - mx); sum += lg[i]; }
    sum += __shfl_xor(sum, 32);
    const float inv = 1.f / sum;
    float o[18];
#pragma unroll
    for (int i = 0; i < 18; ++i) o[i] = 0.f;
#pragma unroll
    for (int i4 = 0; i4 < 4; ++i4) {
      const int w = 4 * part + i4;
      const float p = (t - 2 * w >= 0) ? rbf(lg[i4] * inv) : 0.f;
      const lds_cptr vp = ldsb + (slot - 2 * w) * K1_QP + 160 + head * 40;
#pragma unroll
      for (int i = 0; i < 5; ++i) { const u32x2 u = *(const LAS u32x2*)(vp + 8 * i);
        const float v0 = lo_f(u[0]), v1 = hi_f(u[0]); o[4 * i] += (t - 2 * w >= 0) ? p * v0 : 0.f; o[4 * i + 1] += (t - 2 * w >= 0) ? p * v1 : 0.f;
        if (i < 4) { const float v2 = lo_f(u[1]), v3 = hi_f(u[1]); o[4 * i + 2] += (t - 2 * w >= 0) ? p * v2 : 0.f; o[4 * i + 3] += (t - 2 * w >= 0) ? p * v3 : 0.f; } }
    }
    KT(27);
    float r10[10];
#pragma unroll
    for (int i = 0; i < 10; ++i) { float a = o[i], b = (i < 8) ? o[10 + i] : 0.f; asm volatile("" : "+v"(a), "+v"(b)); const float send = part ? a : b, keep = part ? b : a; r10[i] = keep + __shfl_xor(send, 32); }
    asm volatile("s_waitcnt lgkmcnt(0)\n\ts_barrier" ::: "memory");
    lds_ptr op = (lds_ptr)lds + tok * 152 + (18 * head + 10 * part) * 2;
#pragma unroll
    for (int i = 0; i < 5; ++i) if (i < 4 || part == 0) *(LAS unsigned*)(op + 4 * i) = cvtpk(r10[2 * i], r10[2 * i + 1]);
  }
  asm volatile("s_waitcnt lgkmcnt(0)\n\ts_barrier" ::: "memory");
  for (int i = tid; i < 64 * 18; i += 512) {
    const int tok = i / 18, qd = i - tok * 18;
    const u32x2 v = *(const LAS u32x2*)(ldsb + tok * 152 + qd * 8);
    const int k = 72 * hg + 4 * qd, ks = k >> 4, jh = (k >> 3) & 1, hh = (k >> 2) & 1;
    const int tb = 2 * mt + (tok >> 5), r = tok & 31;
    *(u32x2*)(of + ((size_t)(tb * NKS + ks) * 64 + hh * 32 + r) * 16 + jh * 8) = v;
  }
  KT(28);
  STAMP1(3);
}

struct Dma1 { const char* gb[7]; unsigned inc[7]; unsigned lo[7]; unsigned hf[7]; unsigned voff[7]; };
DI unsigned nxt(unsigned so) { so += HSLOT; return so >= NHS * HSLOT ? so - NHS * HSLOT : so; }

template <int NBK> DI void phase1(Dma1& dm, const unsigned l0, lds_cptr ldsb, const unsigned rdA, const unsigned rdX, f32x16 (&acc)[2 * NBK]) {
  constexpr int CNT = 4 * NBK, LA = 6, RING = (NBK == 5) ? 10 : 8, ND = (NBK == 5) ? 4 : 7;
  auto dma_piece = [&](int k, unsigned so0, unsigned so1) {
    glds16s(dm.voff[k], dm.gb[k], l0 + (dm.hf[k] ? so1 : so0) + dm.lo[k]);
    dm.gb[k] += dm.inc[k];
  };
  {
    unsigned so = 0;
    for (int st = 0; st < 3; ++st) {
#pragma unroll
      for (int k = 0; k < ND; ++k) dma_piece(k, so, so + HSLOT);
      so += 2 * HSLOT;
    }
  }
  bf16x8 fr[RING]; bf16x8 xq[2];
  auto rd = [&](int n, unsigned so0, unsigned so1) -> bf16x8 {
    const int half = n / (2 * NBK), t = n % (2 * NBK), b = t >> 1, mat = t & 1;
    lds_cptr p = ldsb + ((half ? so1 : so0) + rdA);
    return trfrag(p, mat * 9216 + b * 64, mat * 9216 + b * 64 + 4 * 576);
  };
  auto rdx = [&](unsigned so) -> bf16x8 { return *(const LAS bf16x8*)(ldsb + (so + rdX)); };
  wait_bar<2 * ND>();
  STAMP(1);
  unsigned c0 = 0, c1 = HSLOT;
  xq[0] = rdx(c0);
  SBAR();
#pragma unroll
  for (int n = 0; n < LA; ++n) { fr[n] = rd(n, c0, c1); SBAR(); }
  unsigned p0 = 0, p1 = 0;
#pragma unroll 1
  for (int st = 0; st < 18; ++st) {
    const unsigned n0 = nxt(c1), n1 = nxt(n0);
    const unsigned i0 = nxt(nxt(nxt(n1))), i1 = nxt(i0);
#pragma unroll
    for (int n = 0; n < CNT; ++n) {
      if (NBK == 5 && n == CNT - LA && st >= 1 && st < 16) { dma_piece(0, p0, p1); dma_piece(1, p0, p1); dma_piece(2, p0, p1); dma_piece(3, p0, p1); }
      if (n == CNT - LA) { if (st >= 16) wait_bar<0>(); else wait_bar<ND>(); xq[0] = rdx(n0); }
      if (n == 2 * NBK - LA) xq[1] = rdx(c1);
      bf16x8 xcur = xq[n / (2 * NBK)];
      if (n + LA < CNT) fr[(n + LA) % RING] = rd(n + LA, c0, c1);
      else fr[(n + LA) % RING] = rd(n + LA - CNT, n0, n1);
      if (NBK != 5 && n == CNT - LA && st < 15) {
#pragma unroll
        for (int k = 0; k < 7; ++k) dma_piece(k, i0, i1);
      }
      acc[n % (2 * NBK)] = MFMA(fr[n % RING], xcur, acc[n % (2 * NBK)]);
      SBAR();
    }
    c0 = n0; c1 = n1; p0 = i0; p1 = i1;
  }
}

__global__ __launch_bounds__(512, 2) void k_mlp(const char* __restrict__ xf, const char* __restrict__ of, const char* __restrict__ wg, const char* __restrict__ wu,
                                                const char* __restrict__ wd, const char* __restrict__ wo, unsigned short* __restrict__ part) {
  extern __shared__ __attribute__((aligned(16))) char lds[];
  STAMP(0);
  const int lane = threadIdx.x & 63;
  const unsigned wid = __builtin_amdgcn_readfirstlane(threadIdx.x >> 6);
  const unsigned tg = wid & 3, fh = wid >> 2;
  const unsigned bx = blockIdx.x, xcd = bx & 7, kk = bx >> 3;
  const unsigned c = xcd * 2 + (kk & 1), mt = kk >> 1;
  const unsigned l0 = (unsigned)(uintptr_t)lds;
  const lds_cptr ldsb = (lds_cptr)lds;
  Dma1 dm;
#pragma unroll
  for (int k = 0; k < 7; ++k) {
    unsigned j = wid >= 4 ? (wid - 4) + 4 * k : 28 + wid + 4 * k; if (j >= 44) j = 43;
    const unsigned half = j >= 22 ? 1u : 0u, jj = j - 22 * half;
    dm.hf[k] = half;
    if (jj < 18) {
      const unsigned mat = jj >= 9 ? 1u : 0u, pj = jj - 9 * mat;
      const unsigned g = pj * 64 + lane, row = g / 36, ch = g - row * 36;
      dm.voff[k] = row * (F_ * 2) + ch * 16;
      dm.gb[k] = (mat ? wu : wg) + 576 * c + (size_t)(16 * half) * (F_ * 2);
      dm.inc[k] = 32 * F_ * 2; dm.lo[k] = jj * 1024;
    } else {
      dm.voff[k] = lane * 16;
      dm.gb[k] = xf + (size_t)((mt * 4 + (jj - 18)) * NKS + half) * 1024;
      dm.inc[k] = 2048; dm.lo[k] = jj * 1024;
    }
  }
  const unsigned h = lane >> 5, blk16 = (lane >> 4) & 1, q4 = (lane & 15) >> 2, p4 = lane & 3;
  const unsigned rdA = (8 * h + q4) * 576 + blk16 * 32 + p4 * 8 + fh * 5 * 64;
  const unsigned rdX = 18432 + tg * 1024 + lane * 16;
  u32x4 own[10];
  const f32x16 zero = {};
#define GLU_PACK(NB) do { \
    _Pragma("unroll") for (int b = 0; b < NB; ++b) _Pragma("unroll") for (int i = 0; i < 8; ++i) { \
        const unsigned gp = cvtpk(fmaxf(acc[2 * b][2 * i], 0.f), fmaxf(acc[2 * b][2 * i + 1], 0.f)), up = cvtpk(acc[2 * b + 1][2 * i], acc[2 * b + 1][2 * i + 1]); \
        own[2 * b + (i >> 2)][i & 3] = cvtpk(lo_f(gp) * lo_f(up), hi_f(gp) * hi_f(up)); } } while (0)
  if (fh == 0) {
    f32x16 acc[10];
#pragma unroll
    for (int i = 0; i < 10; ++i) acc[i] = zero;
    phase1<5>(dm, l0, ldsb, rdA, rdX, acc);
    GLU_PACK(5);
  } else {
    f32x16 acc[8];
#pragma unroll
    for (int i = 0; i < 8; ++i) acc[i] = zero;
    phase1<4>(dm, l0, ldsb, rdA, rdX, acc);
    GLU_PACK(4);
  }
  const unsigned ks0 = (9 * c) >> 2, nko = ((9 * (c + 1)) >> 2) - ks0;
  const unsigned lrow = lane >> 3, ch2 = (lane & 7) ^ (((lrow >> 1) & 1) << 2);
  const unsigned voff2 = lrow * (D_ * 2) + ch2 * 16;
  const char* gp2[6]; unsigned lo2[6], hf2[6];
#pragma unroll
  for (int k = 0; k < 6; ++k) {
    unsigned j = wid >= 4 ? (wid - 4) + 4 * k : 24 + wid + 4 * k; if (j >= 42) j = 41;
    const unsigned row = 8 * j;
    hf2[k] = j >= 22 ? 1u : 0u; lo2[k] = (j - 22 * hf2[k]) * 1024;
    gp2[k] = row < 288 ? wd + (size_t)(288 * c + row) * (D_ * 2) : wo + (size_t)(16 * (ks0 > 33 ? 33 : ks0) + (row - 288)) * (D_ * 2);
  }
  const int nd2 = wid >= 4 ? 6 : (wid < 2 ? 5 : 4);
  auto dma2 = [&](int q, int k) {
    const unsigned hh = (36 + 2 * q) % 7;
    const unsigned s0 = hh * HSLOT, s1 = ((hh + 1) % 7) * HSLOT;
    glds16s(voff2, gp2[k] + q * 128, l0 + (hf2[k] ? s1 : s0) + lo2[k]);
  };
  auto dma2_all = [&](int q) {
#pragma unroll
    for (int k = 0; k < 6; ++k) if (k < 4 || wid >= 4 || (k == 4 && wid < 2)) dma2(q, k);
  };
  wait_bar_l<0>();
  STAMP(2);
  dma2_all(0);
  bf16x8 hf[21];
  { const bf16x8* op = (const bf16x8*)of + (size_t)((mt * 4 + tg) * NKS + (ks0 > 33 ? 33 : ks0)) * 64 + lane;
    hf[18] = op[0]; hf[19] = op[64]; hf[20] = op[128]; }
  const unsigned hxo = 3 * HSLOT + tg * 18432 + lane * 16;
  {
    const lds_ptr hx = (lds_ptr)lds + hxo + fh * 10240;
#pragma unroll
    for (int i = 0; i < 10; ++i) if (i < 8 || fh == 0) *(LAS u32x4*)(hx + i * 1024) = own[i];
    asm volatile("s_waitcnt lgkmcnt(0)\n\ts_barrier" ::: "memory");
  }
#pragma unroll
  for (int s = 0; s < 18; ++s) hf[s] = *(const LAS bf16x8*)(ldsb + hxo + s * 1024);
  if (nko < 3) hf[20] = bf16x8{};
  if (ks0 > 33) { hf[18] = hf[19]; hf[19] = hf[20]; }
  wait_bar_l<0>();
  dma2_all(1); dma2_all(2);
  STAMP(3);
  const unsigned colb = (((4 * fh + 2 * blk16 + (p4 >> 1)) ^ ((q4 >> 1) << 2)) * 16) + 8 * (p4 & 1);
  const lds_cptr rb2 = ldsb + (4 * h + q4) * 128 + colb;
  unsigned short* pp = part + ((size_t)(c * 64 + mt * 4 + tg) * 18 + fh) * 1024 + lane * 8;
  {
    constexpr int LA = 6, RING = 7, CNT = 21;
    bf16x8 fr[RING];
    auto rd2 = [&](int q, int n) -> bf16x8 {
      const int hh = (36 + 2 * q + (n >= 11 ? 1 : 0)) % 7, nn = n >= 11 ? n - 11 : n;
      return trfrag(rb2 + hh * HSLOT, nn * 2048, nn * 2048 + 1024);
    };
#pragma unroll
    for (int n = 0; n < LA; ++n) { fr[n] = rd2(0, n); SBAR(); }
    f32x16 acc[2];
#pragma unroll
    for (int q = 0; q < 9; ++q) {
#pragma unroll
      for (int n = 0; n < CNT; ++n) {
        if (n == CNT - LA && q < 8) {
          if (wid < 4 && q >= 1 && q + 2 < 9) {
#pragma unroll
            for (int k = 0; k < 5; ++k) if (k < 4 || wid < 2) dma2(q + 2, k);
          }
          if (wid >= 4) {
            if (q == 0) wait_bar<6>(); else if (q == 1) wait_bar<8>(); else if (q + 2 < 9) wait_bar<10>(); else wait_bar<4>();
          } else if (wid < 2) {
            if (q == 0) wait_bar<5>(); else if (q + 2 < 9) wait_bar<7>(); else wait_bar<2>();
          } else {
            if (q == 0) wait_bar<4>(); else if (q + 2 < 9) wait_bar<6>(); else wait_bar<2>();
          }
          if (wid >= 4 && q + 3 < 9) {
#pragma unroll
            for (int k = 0; k < 6; ++k) dma2(q + 3, k);
          }
        }
        if (n + LA < CNT) fr[(n + LA) % RING] = rd2(q, n + LA);
        else if (q < 8) fr[(n + LA) % RING] = rd2(q + 1, n + LA - CNT);
        acc[q & 1] = MFMA(fr[n % RING], hf[n], n == 0 ? zero : acc[q & 1]);
        if (n == 3 && q > 0) {
          u32x4 o0, o1;
#pragma unroll
          for (int i = 0; i < 4; ++i) { o0[i] = cvtpk(acc[(q - 1) & 1][2 * i], acc[(q - 1) & 1][2 * i + 1]); o1[i] = cvtpk(acc[(q - 1) & 1][8 + 2 * i], acc[(q - 1) & 1][8 + 2 * i + 1]); }
          PSTORE(pp + (q - 1) * 2048, o0); PSTORE(pp + (q - 1) * 2048 + 512, o1);
        }
        SBAR();
      }
    }
    u32x4 o0, o1;
#pragma unroll
    for (int i = 0; i < 4; ++i) { o0[i] = cvtpk(acc[0][2 * i], acc[0][2 * i + 1]); o1[i] = cvtpk(acc[0][8 + 2 * i], acc[0][8 + 2 * i + 1]); }
    PSTORE(pp + 8 * 2048, o0); PSTORE(pp + 8 * 2048 + 512, o1);
  }
  STAMP(4);
}

__global__ __launch_bounds__(256) void k_reduce(const unsigned short* __restrict__ part, const unsigned short* __restrict__ x, unsigned short* __restrict__ out) {
  const unsigned idx = blockIdx.x * 256 + threadIdx.x;
  const unsigned lane = idx & 63, half = (idx >> 6) & 1, tile = idx >> 7, db = tile % 18, tb = tile / 18;
  u32x4 v[NCH];
#pragma unroll
  for (int c = 0; c < NCH; ++c) v[c] = __builtin_nontemporal_load((const u32x4*)(part + (size_t)c * (S_ * D_) + (size_t)idx * 8));
  const unsigned t = tb * 32 + (lane & 31), d0 = db * 32 + half * 16 + 4 * (lane >> 5);
  const size_t off = (size_t)t * D_ + d0;
  const u32x2 xa = *(const u32x2*)(x + off), xb = *(const u32x2*)(x + off + 8);
  float s[8] = {lo_f(xa[0]), hi_f(xa[0]), lo_f(xa[1]), hi_f(xa[1]), lo_f(xb[0]), hi_f(xb[0]), lo_f(xb[1]), hi_f(xb[1])};
  float a[8] = {0, 0, 0, 0, 0, 0, 0, 0};
#pragma unroll
  for (int c = 0; c < NCH; ++c)
#pragma unroll
    for (int i = 0; i < 4; ++i) { a[2 * i] += lo_f(v[c][i]); a[2 * i + 1] += hi_f(v[c][i]); }
  u32x2 o0, o1;
  o0[0] = cvtpk(s[0] + a[0], s[1] + a[1]); o0[1] = cvtpk(s[2] + a[2], s[3] + a[3]);
  o1[0] = cvtpk(s[4] + a[4], s[5] + a[5]); o1[1] = cvtpk(s[6] + a[6], s[7] + a[7]);
  *(u32x2*)(out + off) = o0; *(u32x2*)(out + off + 8) = o1;
}

extern "C" void kernel_launch(void* const* d_in, const int* in_sizes, int n_in, void* d_out, int out_size, void* d_ws, size_t ws_size, hipStream_t stream) {
  const unsigned short* x = (const unsigned short*)d_in[0];
  const char* wqkv = (const char*)d_in[1];
  const char* wo = (const char*)d_in[2];
  const char* wg = (const char*)d_in[4]; const char* wu = (const char*)d_in[5]; const char* wd = (const char*)d_in[6];
  char* ws = (char*)d_ws;
  char* xf = ws;
  char* of = ws + (4u << 20);
  unsigned short* part = (unsigned short*)(ws + (8u << 20));
  (void)hipFuncSetAttribute((const void*)k_mlp, hipFuncAttributeMaxDynamicSharedMemorySize, LDS_BYTES);
  (void)hipFuncSetAttribute((const void*)k_attn, hipFuncAttributeMaxDynamicSharedMemorySize, LDS_BYTES);
  k_attn<<<256, 512, K1_LDS, stream>>>(x, wqkv, xf, of);
  k_mlp<<<256, 512, LDS_BYTES, stream>>>(xf, of, wg, wu, wd, wo, part);
  k_reduce<<<(S_ * D_ / 8) / 256, 256, 0, stream>>>(part, x, (unsigned short*)d_out);
}
```

```cpp
#include <hip/hip_runtime.h>
#include <hip/hip_bf16.h>
#include <stdint.h>
#include <utility>
#include <type_traits>

using bf16x8 = __attribute__((ext_vector_type(8))) short;
using s16x4  = __attribute__((ext_vector_type(4))) short;
using f32x16 = __attribute__((ext_vector_type(16))) float;
using f32x2  = __attribute__((ext_vector_type(2))) float;
using u32x4  = __attribute__((ext_vector_type(4))) unsigned;
using u32x2  = __attribute__((ext_vector_type(2))) unsigned;
#define LAS __attribute__((address_space(3)))
typedef LAS const char* lds_cptr;
typedef LAS char* lds_ptr;
typedef short v4i16_t __attribute__((ext_vector_type(4)));
typedef __bf16 bf16x2_t __attribute__((ext_vector_type(2)));
#define DI __device__ __forceinline__
#define SBAR() __builtin_amdgcn_sched_barrier(0)

constexpr int S_ = 2048, D_ = 576, F_ = 4608;
constexpr int NKS = 36;
constexpr int NCH = 16;
constexpr int SL = 26624;
constexpr int NSL = 6;
constexpr int LDS_BYTES = 163840;
constexpr int QP = 232;

DI unsigned cvtpk(float lo, float hi) { f32x2 v = {lo, hi}; bf16x2_t b = __builtin_convertvector(v, bf16x2_t); return __builtin_bit_cast(unsigned, b); }
DI float rbf(float f) { return __uint_as_float(cvtpk(f, 0.f) << 16); }
DI float lo_f(unsigned p) { return __uint_as_float(p << 16); }
DI float hi_f(unsigned p) { return __uint_as_float(p & 0xffff0000u); }
DI float dot2(unsigned a, unsigned b, float c) { return __builtin_amdgcn_fdot2_f32_bf16(__builtin_bit_cast(bf16x2_t, a), __builtin_bit_cast(bf16x2_t, b), c, false); }
DI s16x4 vtr(lds_cptr p) { return __builtin_bit_cast(s16x4, __builtin_amdgcn_ds_read_tr16_b64_v4i16((LAS v4i16_t*)p)); }
DI bf16x8 trfrag(lds_cptr p, int o0, int o1) { s16x4 a = vtr(p + o0); SBAR(); s16x4 b = vtr(p + o1); return __builtin_shufflevector(a, b, 0, 1, 2, 3, 4, 5, 6, 7); }
DI void glds16s(unsigned voff, const void* sbase, unsigned lds_base) {
  asm volatile("s_mov_b32 m0, %2\n\ts_nop 0\n\tglobal_load_lds_dwordx4 %0, %1" :: "v"(voff), "s"(sbase), "s"(lds_base) : "memory", "m0");
}
#define PSTORE(p, v) asm volatile("global_store_dwordx4 %0, %1, off sc0 sc1\n\ts_nop 1" :: "v"(p), "v"(v) : "memory")
#define MFMA(a, b, c) __builtin_amdgcn_mfma_f32_32x32x16_bf16((a), (b), (c), 0, 0, 0)
template <int N> DI void wait_bar() { asm volatile("s_waitcnt vmcnt(%0)\n\ts_barrier" :: "i"(N) : "memory"); }
template <int N> DI void wait_bar_l() { asm volatile("s_waitcnt vmcnt(%0) lgkmcnt(0)\n\ts_barrier" :: "i"(N) : "memory"); }
DI void bar_l() { asm volatile("s_waitcnt lgkmcnt(0)\n\ts_barrier" ::: "memory"); }
template <int C> DI lds_cptr vaddc(unsigned v) { unsigned r; if (C == 0) r = v; else asm volatile("v_add_u32_e32 %0, %2, %1" : "=v"(r) : "v"(v), "i"(C)); return (lds_cptr)(size_t)r; }
DI unsigned fresh_lane() { unsigned l; asm volatile("v_mbcnt_lo_u32_b32 %0, -1, 0\n\tv_mbcnt_hi_u32_b32 %0, -1, %0" : "=v"(l)); return l; }

#define STAMP(i)

__global__ __launch_bounds__(64) void k_ln(const unsigned short* __restrict__ x, char* __restrict__ xf, unsigned* __restrict__ flags,
                                           const char* __restrict__ wqkv, char* __restrict__ wqp) {
  {
    u32x2 w[18];
#pragma unroll
    for (int i = 0; i < 18; ++i) {
      const unsigned id = (blockIdx.x * 18 + i) * 64 + threadIdx.x, rowg = id >> 5, qd = id & 31, cch = rowg / 576, row = rowg - cch * 576, pt = qd / 9;
      w[i] = u32x2{0u, 0u};
      if (qd < 27) w[i] = *(const u32x2*)(wqkv + (size_t)row * 3456 + pt * 1152 + 72 * cch + (qd - 9 * pt) * 8);
    }
#pragma unroll
    for (int i = 0; i < 18; ++i) {
      const unsigned id = (blockIdx.x * 18 + i) * 64 + threadIdx.x, rowg = id >> 5, qd = id & 31;
      *(u32x2*)(wqp + (size_t)rowg * 256 + (((qd >> 3) ^ (rowg & 3)) * 64) + (qd & 7) * 8) = w[i];
    }
  }
  const int lane = threadIdx.x, j = lane & 7, t = blockIdx.x * 8 + (lane >> 3);
  if (lane == 0) { flags[blockIdx.x * 32] = 0u; if (blockIdx.x < 32) flags[(256 + blockIdx.x) * 32] = 0u; }
  const u32x4* src = (const u32x4*)(x + (size_t)t * D_) + j;
  u32x4 v[9];
#pragma unroll
  for (int i = 0; i < 9; ++i) v[i] = src[8 * i];
  float s = 0, q = 0;
#pragma unroll
  for (int i = 0; i < 9; ++i)
#pragma unroll
    for (int e = 0; e < 4; ++e) { s = dot2(v[i][e], 0x3f803f80u, s); q = dot2(v[i][e], v[i][e], q); }
#pragma unroll
  for (int o = 1; o < 8; o <<= 1) { s += __shfl_xor(s, o); q += __shfl_xor(q, o); }
  const float m = s * (1.f / D_);
  const float var = fmaxf(q * (1.f / D_) - m * m, 0.f);
  const float mb = rbf(m), vb = rbf(var);
  const float ri = 1.f / rbf(sqrtf(rbf(vb + 1e-5f)));
  const f32x2 mb2 = {mb, mb}, ri2 = {ri, ri};
  char* gdst = xf + (size_t)((t >> 5) * NKS) * 1024 + (t & 31) * 16 + (j & 1) * 512 + (j >> 1) * 1024;
#pragma unroll
  for (int i = 0; i < 9; ++i) {
    u32x4 o;
#pragma unroll
    for (int e = 0; e < 4; ++e) { f32x2 a = {lo_f(v[i][e]), hi_f(v[i][e])}; a -= mb2; const unsigned d = cvtpk(a[0], a[1]); f32x2 b = {lo_f(d), hi_f(d)}; b *= ri2; o[e] = cvtpk(b[0], b[1]); }
    *(u32x4*)(gdst + i * 4096) = o;
  }
}

template <class F, int... I> DI void for_each_int(F&& f, std::integer_sequence<int, I...>) { (f(std::integral_constant<int, I>{}), ...); }
struct Ptrs { const char* wg; const char* wu; const char* xm; const char* wq; };

DI void dma_piece(const Ptrs& P, unsigned p, unsigned hh, unsigned ldsdst) {
  const unsigned l = fresh_lane();
  if (p < 18) {
    const unsigned mat = p >= 9 ? 1u : 0u, pj = p - 9 * mat;
    const unsigned g = l + 64 * pj, row = (g * 1821u) >> 16;
    glds16s(row * 8640u + g * 16u, (mat ? P.wu : P.wg) + (size_t)hh * (16 * F_ * 2), ldsdst + p * 1024);
  } else if (p < 22) {
    glds16s(l * 16u, P.xm + (size_t)((p - 18) * NKS + hh) * 1024, ldsdst + p * 1024);
  } else {
    glds16s(l * 16u, P.wq + (size_t)hh * 4096 + (p - 22) * 1024, ldsdst + p * 1024);
  }
}

template <int FH, class F> DI void phase1(F&& tail_dma, const Ptrs& P, const unsigned w4, const unsigned l0, lds_cptr ldsb, const unsigned rdA, const unsigned rdX, const unsigned (&rdQ)[3], f32x16 (&acc)[11]) {
  constexpr int LA = 5, RING = 6;
  auto issue_odd = [&](unsigned hh, unsigned so) {
#pragma unroll
    for (int k = 0; k < 7; ++k) if (k < 6 || w4 < 2) dma_piece(P, w4 + 4 * k, hh, l0 + so);
  };
  auto issue_evenB = [&](unsigned hh, unsigned so) {
    if (w4 < 2) dma_piece(P, 20 + w4, hh, l0 + so); else { dma_piece(P, 18 + 2 * w4, hh, l0 + so); dma_piece(P, 19 + 2 * w4, hh, l0 + so); }
  };
  auto issue_evenA = [&](unsigned hh, unsigned so) {
#pragma unroll
    for (int k = 0; k < 5; ++k) dma_piece(P, w4 + 4 * k, hh, l0 + so);
  };
  if (FH) { issue_evenB(0, 0); issue_odd(1, SL); issue_evenB(2, 2 * SL); issue_odd(3, 3 * SL); issue_evenB(4, 4 * SL); }
  else { issue_evenA(0, 0); issue_evenA(2, 2 * SL); }
  bf16x8 fr[RING]; bf16x8 xq[2];
  constexpr int NGU = FH ? 8 : 10;
  lds_cptr aG = nullptr, aQ[3] = {nullptr, nullptr, nullptr};
  const unsigned rA = l0 + rdA, rX = l0 + rdX, rQ0 = l0 + rdQ[0], rQ1 = l0 + rdQ[1], rQ2 = l0 + rdQ[2];
#define SET_SLOT(slot) do { aG = vaddc<(slot) * SL>(rA); aQ[0] = vaddc<(slot) * SL>(rQ0); if (FH) { aQ[1] = vaddc<(slot) * SL>(rQ1); aQ[2] = vaddc<(slot) * SL>(rQ2); } } while (0)
#define RD(dst, n) do { constexpr int nn_ = (n) % 66, slot_ = nn_ / 11, t_ = nn_ % 11; if (t_ == 0) SET_SLOT(slot_); \
    if (t_ < NGU) dst = trfrag(aG, (t_ & 1) * 9216 + (t_ >> 1) * 64, (t_ & 1) * 9216 + (t_ >> 1) * 64 + 4 * 576); else dst = trfrag(aQ[t_ < NGU ? 0 : t_ - NGU], 0, 4 * 256); } while (0)
#define RDX(slot) (*(const LAS bf16x8*)vaddc<(slot) * SL>(rX))
  if (FH) { if (w4 < 2) wait_bar<9>(); else wait_bar<10>(); } else wait_bar<5>();
  STAMP(1);
  xq[0] = RDX(0);
  SBAR();
  RD(fr[0], 0); SBAR(); RD(fr[1], 1); SBAR(); RD(fr[2], 2); SBAR(); RD(fr[3], 3); SBAR(); RD(fr[4], 4); SBAR();
#pragma unroll 1
  for (int it = 0; it < 6; ++it) {
    auto step = [&](auto n_) {
      constexpr int n = decltype(n_)::value;
      constexpr int S3 = n / 22, m = n % 22;
      const int st = 3 * it + S3;
      if (m == 22 - LA) {
        if (!FH && st <= 15) issue_evenA(2 * st + 4, ((2 * S3 + 4) % 6) * SL);
        if (st < 17) {
          if (st == 16) wait_bar<0>();
          else if (FH) { if (w4 < 2) wait_bar<1>(); else wait_bar<2>(); }
          else wait_bar<5>();
        }
        xq[0] = RDX((2 * S3 + 2) % 6);
        if (FH) { if (st <= 15) issue_odd(2 * st + 5, ((2 * S3 + 5) % 6) * SL); if (st <= 14) issue_evenB(2 * st + 6, ((2 * S3 + 6) % 6) * SL); }
        if (S3 == 1 && st == 16) tail_dma();
      }
      if (m == 11 - LA) xq[1] = RDX(2 * S3 + 1);
      const bf16x8 xcur = xq[m / 11];
      RD(fr[(n + LA) % RING], n + LA);
      acc[m % 11] = MFMA(fr[n % RING], xcur, acc[m % 11]);
      SBAR();
    };
    for_each_int(step, std::make_integer_sequence<int, 66>{});
  }
#undef RD
#undef RDX
#undef SET_SLOT
}

__global__ __launch_bounds__(512, 2) void k_main(const char* __restrict__ xf, const char* __restrict__ wqkv, const char* __restrict__ wg, const char* __restrict__ wu,
                                                 const char* __restrict__ wd, const char* __restrict__ wo, unsigned short* __restrict__ part,
                                                 char* __restrict__ halo, unsigned* __restrict__ flags,
                                                 const unsigned short* __restrict__ x, unsigned short* __restrict__ out) {
  extern __shared__ __attribute__((aligned(16))) char lds[];
  STAMP(0);
  const int tid = threadIdx.x, lane = tid & 63;
  const unsigned wid = __builtin_amdgcn_readfirstlane(tid >> 6);
  const unsigned tg = wid & 3, fh = wid >> 2;
  const unsigned bx = blockIdx.x, xcd = bx & 7, kk = bx >> 3;
  const unsigned c = xcd * 2 + (kk & 1), mt = kk >> 1;
  const unsigned l0 = (unsigned)(uintptr_t)lds;
  const lds_cptr ldsb = (lds_cptr)lds;
  Ptrs P;
  P.wg = wg + 576 * c; P.wu = wu + 576 * c; P.xm = xf + (size_t)(mt * 4) * NKS * 1024; P.wq = wqkv + (size_t)c * (576 * 256);
  const unsigned h = lane >> 5, blk16 = (lane >> 4) & 1, q4 = (lane & 15) >> 2, p4 = lane & 3;
  const unsigned rdA = (8 * h + q4) * 576 + blk16 * 32 + p4 * 8 + fh * 5 * 64;
  const unsigned rdX = 18432 + tg * 1024 + lane * 16;
  unsigned rdQ[3];
#pragma unroll
  for (int i = 0; i < 3; ++i) { const unsigned t = fh ? i + 1 : 0; rdQ[i] = 22528 + (8 * h + q4) * 256 + ((t ^ q4) * 64) + blk16 * 32 + p4 * 8; }
  const unsigned lrow = lane >> 3, ch2 = (lane & 7) ^ (((lrow >> 1) & 1) << 2);
  const unsigned voff2 = lrow * (D_ * 2) + ch2 * 16;
  auto dma2j = [&](int q, unsigned j) {
    const int hh = (2 * q) % 6;
    const unsigned so = j >= 22 ? ((hh + 1) % 6) * SL + (j - 22) * 1024 : hh * SL + j * 1024;
    if (j < 36) glds16s(voff2, wd + (size_t)(288 * c + 8 * j) * (D_ * 2) + q * 128, l0 + so);
    else { const unsigned l = fresh_lane(), lr = l >> 3; unsigned wr = 36 * c + 8 * (j - 36) + lr; if (wr > 575) wr = 575;
      glds16s(wr * (D_ * 2) + (((l & 7) ^ (((lr >> 1) & 1) << 2)) * 16), wo + q * 128, l0 + so); }
  };
  auto dma2_A = [&](int q) {
#pragma unroll
    for (int k = 0; k < 5; ++k) dma2j(q, tg + 4 * k);
  };
  auto dma2_Bodd = [&](int q) {
#pragma unroll
    for (int k = 0; k < 5; ++k) dma2j(q, 22 + tg + 4 * k);
  };
  auto dma2_Beven = [&](int q) { if (tg < 2) dma2j(q, 20 + tg); };
  auto dma2_all = [&](int q) { if (fh) { dma2_Beven(q); dma2_Bodd(q); } else dma2_A(q); };
  const f32x16 zero = {};
  f32x16 acc[11];
#pragma unroll
  for (int i = 0; i < 11; ++i) acc[i] = zero;
  if (fh) phase1<1>([&]() { dma2_all(0); }, P, tg, l0, ldsb, rdA, rdX, rdQ, acc); else phase1<0>([&]() { dma2_all(0); }, P, tg, l0, ldsb, rdA, rdX, rdQ, acc);
  u32x4 own[10];
#pragma unroll
  for (int b = 0; b < 5; ++b) if (b < 4 || fh == 0)
#pragma unroll
    for (int i = 0; i < 8; ++i) {
      const unsigned gp = cvtpk(fmaxf(acc[2 * b][2 * i], 0.f), fmaxf(acc[2 * b][2 * i + 1], 0.f)), up = cvtpk(acc[2 * b + 1][2 * i], acc[2 * b + 1][2 * i + 1]);
      own[2 * b + (i >> 2)][i & 3] = cvtpk(lo_f(gp) * lo_f(up), hi_f(gp) * hi_f(up));
    }
  if (wid == 7) {
    char* hb = halo + (size_t)(mt * 16 + c) * 4096 + ((lane & 15) * 144);
    if (lane & 16) {
#pragma unroll
      for (int t = 1; t < 4; ++t)
#pragma unroll
        for (int g = 0; g < 4; ++g) {
          const int c0 = 32 * t + 8 * g;
          if (c0 + 4 >= 36 && c0 < 108) {
            const u32x2 w = {cvtpk(acc[7 + t][4 * g], acc[7 + t][4 * g + 1]), cvtpk(acc[7 + t][4 * g + 2], acc[7 + t][4 * g + 3])};
            const int cc = c0 + 4 * (int)h;
            if (cc >= 36 && cc < 108) asm volatile("global_store_dwordx2 %0, %1, off sc0 sc1" :: "v"(hb + (cc - 36) * 2), "v"(w) : "memory");
          }
        }
    }
  }
  bar_l();
  STAMP(2);
  if (wid == 7) {
    asm volatile("s_waitcnt vmcnt(0)" ::: "memory");
    if (lane == 0) __hip_atomic_store(flags + (mt * 16 + c) * 32, 1u, __ATOMIC_RELAXED, __HIP_MEMORY_SCOPE_AGENT);
  }
  constexpr int HX = 2 * SL, QK = HX + 73728;
  const unsigned hxo = HX + tg * 18432 + lane * 16;
  {
    const lds_ptr hx = (lds_ptr)lds + hxo + fh * 10240;
#pragma unroll
    for (int i = 0; i < 10; ++i) if (i < 8 || fh == 0) *(LAS u32x4*)(hx + i * 1024) = own[i];
    lds_ptr d = (lds_ptr)lds + QK + (16 + tg * 32 + (lane & 31)) * QP + 8 * h;
    auto put = [&](const f32x16& a, int t) {
#pragma unroll
      for (int g = 0; g < 4; ++g) if (32 * t + 8 * g < 108) {
        const u32x2 w = {cvtpk(a[4 * g], a[4 * g + 1]), cvtpk(a[4 * g + 2], a[4 * g + 3])};
        if (32 * t + 8 * g + 4 < 108 || h == 0) *(LAS u32x2*)(d + (32 * t + 8 * g) * 2) = w;
      }
    };
    if (fh) { put(acc[8], 1); put(acc[9], 2); put(acc[10], 3); } else put(acc[10], 0);
  }
  if (mt > 0 && wid == 0) {
    const unsigned* f = flags + ((mt - 1) * 16 + c) * 32;
    unsigned spins = 0;
    while (__builtin_amdgcn_readfirstlane(__hip_atomic_load(f, __ATOMIC_RELAXED, __HIP_MEMORY_SCOPE_AGENT)) == 0u && ++spins < (1u << 22)) __builtin_amdgcn_s_sleep(1);
    asm volatile("" ::: "memory");
    const char* hb = halo + (size_t)((mt - 1) * 16 + c) * 4096;
#pragma unroll
    for (int i = 0; i < 3; ++i) {
      const unsigned id = lane + 64 * i;
      if (id < 144) {
        u32x4 v;
        asm volatile("global_load_dwordx4 %0, %1, off sc0 sc1\n\ts_waitcnt vmcnt(0)" : "=&v"(v) : "v"(hb + id * 16) : "memory");
        const unsigned tk = id / 9, ck = id - tk * 9;
        lds_ptr d = (lds_ptr)lds + QK + tk * QP + 72 + ck * 16;
        *(LAS u32x2*)d = u32x2{v[0], v[1]}; *(LAS u32x2*)(d + 8) = u32x2{v[2], v[3]};
      }
    }
  }
  if (mt == 0 && wid == 0) {
#pragma unroll
    for (int i = 0; i < 8; ++i) { const unsigned id = lane + 64 * i; if (id < 16 * (QP / 8)) *(LAS u32x2*)((lds_ptr)lds + QK + id * 8) = u32x2{0u, 0u}; }
  }
  bar_l();
  STAMP(3);
  bf16x8 hf[21];
#pragma unroll
  for (int s = 0; s < 18; ++s) hf[s] = *(const LAS bf16x8*)(ldsb + hxo + s * 1024);
  STAMP(5);
  float r10[10];
  const int tok = (tid & 31) + 32 * ((tid >> 6) & 3), wp = (tid >> 5) & 1; const int head = tid >> 8;
  {
    const int t = 128 * mt + tok, slot = 16 + tok;
    const int ox = head * 40, oy = head ? 36 : 32;
    const lds_cptr qp = ldsb + QK + slot * QP;
    unsigned qv[9];
#pragma unroll
    for (int i = 0; i < 4; ++i) { const u32x2 u = *(const LAS u32x2*)(qp + ox + 8 * i); qv[2 * i] = u[0]; qv[2 * i + 1] = u[1]; }
    qv[8] = *(const LAS unsigned*)(qp + oy);
    float lg[4];
#pragma unroll
    for (int i4 = 0; i4 < 4; ++i4) {
      const int w = 4 * wp + i4;
      const lds_cptr kp = qp - 2 * w * QP + 72;
      float a = 0;
#pragma unroll
      for (int i = 0; i < 4; ++i) { const u32x2 u = *(const LAS u32x2*)(kp + ox + 8 * i); a = dot2(qv[2 * i], u[0], a); a = dot2(qv[2 * i + 1], u[1], a); }
      a = dot2(qv[8], *(const LAS unsigned*)(kp + oy), a);
      lg[i4] = (t - 2 * w >= 0) ? rbf(rbf(a) * 0.23570226039551584f) : -1e9f;
    }
    float mx = fmaxf(fmaxf(lg[0], lg[1]), fmaxf(lg[2], lg[3]));
    mx = fmaxf(mx, __shfl_xor(mx, 32));
    float sum = 0;
#pragma unroll
    for (int i = 0; i < 4; ++i) { lg[i] = __expf(lg[i] - mx); sum += lg[i]; }
    sum += __shfl_xor(sum, 32);
    const float inv = 1.f / sum;
    f32x2 o[9];
#pragma unroll
    for (int i = 0; i < 9; ++i) o[i] = f32x2{0.f, 0.f};
#pragma unroll
    for (int i4 = 0; i4 < 4; ++i4) {
      const int w = 4 * wp + i4;
      const float p = rbf(lg[i4] * inv); const f32x2 p2 = {p, p};
      const lds_cptr vp = qp - 2 * w * QP + 144;
#pragma unroll
      for (int i = 0; i < 4; ++i) { const u32x2 u = *(const LAS u32x2*)(vp + ox + 8 * i);
        o[2 * i] += p2 * f32x2{lo_f(u[0]), hi_f(u[0])}; o[2 * i + 1] += p2 * f32x2{lo_f(u[1]), hi_f(u[1])}; }
      const unsigned y = *(const LAS unsigned*)(vp + oy);
      o[8] += p2 * f32x2{lo_f(y), hi_f(y)};
    }
#pragma unroll
    for (int i = 0; i < 5; ++i) {
      f32x2 a = (i < 4) ? o[i] : o[8], b = (i < 4) ? o[4 + i] : f32x2{0.f, 0.f};
      asm volatile("" : "+v"(a), "+v"(b));
      const f32x2 send = wp ? a : b, keep = wp ? b : a;
      r10[2 * i] = keep[0] + __shfl_xor(send[0], 32); r10[2 * i + 1] = keep[1] + __shfl_xor(send[1], 32);
    }
  }
  bar_l();
  {
    lds_ptr op = (lds_ptr)lds + HX + tok * 104 + head * 36;
#pragma unroll
    for (int i = 0; i < 4; ++i) *(LAS unsigned*)(op + 4 * (head + 4 * wp + i)) = cvtpk(r10[2 * i], r10[2 * i + 1]);
    if (wp == 0) *(LAS unsigned*)(op + (head ? 0 : 32)) = cvtpk(r10[8], r10[9]);
    if (head == 1 && wp == 1) { lds_ptr z = (lds_ptr)lds + HX + tok * 104 + 72; *(LAS u32x2*)z = u32x2{0u, 0u}; *(LAS u32x2*)(z + 8) = u32x2{0u, 0u}; *(LAS u32x2*)(z + 16) = u32x2{0u, 0u}; }
  }
  bar_l();
  STAMP(6);
#pragma unroll
  for (int s = 0; s < 3; ++s) {
    const lds_cptr p = ldsb + HX + (tg * 32 + (lane & 31)) * 104 + (16 * s + 4 * h) * 2;
    const u32x2 a = *(const LAS u32x2*)p, b = *(const LAS u32x2*)(p + 16);
    hf[18 + s] = __builtin_bit_cast(bf16x8, u32x4{a[0], a[1], b[0], b[1]});
  }
  wait_bar_l<0>();
  dma2_all(1); dma2_all(2);
  STAMP(7);
  const unsigned colb = (((4 * fh + 2 * blk16 + (p4 >> 1)) ^ ((q4 >> 1) << 2)) * 16) + 8 * (p4 & 1);
  const lds_cptr rb2 = ldsb + (4 * h + q4) * 128 + colb;
  unsigned short* pp = part + (((size_t)(mt * 4 + tg) * 18 + fh) * 32 + c) * 512 + lane * 8;
  {
    constexpr int LA = 6, RING = 7, CNT = 21;
    bf16x8 fr[RING];
    auto rd2 = [&](int q, int n) -> bf16x8 {
      const int hh = (2 * q + (n >= 11 ? 1 : 0)) % 6, nn = n >= 11 ? n - 11 : n;
      return trfrag(rb2 + hh * SL, nn * 2048, nn * 2048 + 1024);
    };
#pragma unroll
    for (int n = 0; n < LA; ++n) { fr[n] = rd2(0, n); SBAR(); }
    f32x16 acc2[2];
#pragma unroll
    for (int q = 0; q < 9; ++q) {
#pragma unroll
      for (int n = 0; n < CNT; ++n) {
        if (n == CNT - LA && q < 8) {
          if (!fh && q >= 1 && q + 2 < 9) dma2_A(q + 2);
          if (fh) {
            if (q == 0) { if (tg < 2) wait_bar<6>(); else wait_bar<5>(); }
            else if (q < 7) { if (tg < 2) wait_bar<3>(); else wait_bar<2>(); }
            else wait_bar<2>();
          } else {
            if (q == 0) wait_bar<5>(); else if (q < 7) wait_bar<7>(); else wait_bar<2>();
          }
          if (fh) { if (q >= 1 && q + 2 < 9) dma2_Bodd(q + 2); if (q + 3 < 9) dma2_Beven(q + 3); }
          if (q == 7 && tid == 64) __hip_atomic_fetch_add(flags + (256 + mt) * 32, 1u, __ATOMIC_RELAXED, __HIP_MEMORY_SCOPE_AGENT);
        }
        if (n + LA < CNT) fr[(n + LA) % RING] = rd2(q, n + LA);
        else if (q < 8) fr[(n + LA) % RING] = rd2(q + 1, n + LA - CNT);
        acc2[q & 1] = MFMA(fr[n % RING], hf[n], n == 0 ? zero : acc2[q & 1]);
        if (n == 3 && q > 0) {
          u32x4 o0, o1;
#pragma unroll
          for (int i = 0; i < 4; ++i) { o0[i] = cvtpk(acc2[(q - 1) & 1][2 * i], acc2[(q - 1) & 1][2 * i + 1]); o1[i] = cvtpk(acc2[(q - 1) & 1][8 + 2 * i], acc2[(q - 1) & 1][8 + 2 * i + 1]); }
          PSTORE(pp + (q - 1) * 32768, o0); PSTORE(pp + (q - 1) * 32768 + 8192, o1);
        }
        SBAR();
      }
    }
    u32x4 o0, o1;
#pragma unroll
    for (int i = 0; i < 4; ++i) { o0[i] = cvtpk(acc2[0][2 * i], acc2[0][2 * i + 1]); o1[i] = cvtpk(acc2[0][8 + 2 * i], acc2[0][8 + 2 * i + 1]); }
    PSTORE(pp + 8 * 32768, o0); PSTORE(pp + 8 * 32768 + 8192, o1);
  }
  STAMP(8);
  asm volatile("s_waitcnt vmcnt(0)" ::: "memory");
  __builtin_amdgcn_s_barrier();
  if (tid == 0) __hip_atomic_fetch_add(flags + (272 + mt) * 32, 1u, __ATOMIC_RELAXED, __HIP_MEMORY_SCOPE_AGENT);
  {
    const unsigned* cn = flags + ((wid < 6 ? 256 : 272) + mt) * 32;
    unsigned spins = 0;
    while (__builtin_amdgcn_readfirstlane(__hip_atomic_load(cn, __ATOMIC_RELAXED, __HIP_MEMORY_SCOPE_AGENT)) < 16u && ++spins < (1u << 22)) __builtin_amdgcn_s_sleep(1);
    asm volatile("" ::: "memory");
  }
  STAMP(9);
  {
    const unsigned wt = c >> 1, tb = mt * 4 + (wt & 3), hf2 = c & 1;
    auto reduce1 = [&](unsigned r, unsigned ln, u32x4 (&v)[NCH], u32x2& xa, u32x2& xb) {
      const unsigned db = 2 * r + (wt >> 2);
      const unsigned short* src = part + ((size_t)(tb * 18 + db) * 2 + hf2) * 8192 + ln * 8;
#pragma unroll
      for (int cc = 0; cc < NCH; ++cc) asm volatile("global_load_dwordx4 %0, %1, off sc0 sc1" : "=v"(v[cc]) : "v"(src + cc * 512) : "memory");
      const size_t off = (size_t)(tb * 32 + (ln & 31)) * D_ + db * 32 + hf2 * 16 + 4 * (ln >> 5);
      xa = *(const u32x2*)(x + off); xb = *(const u32x2*)(x + off + 8);
    };
#define WAITV(v) asm volatile("s_waitcnt vmcnt(0)" : "+v"(v[0]), "+v"(v[1]), "+v"(v[2]), "+v"(v[3]), "+v"(v[4]), "+v"(v[5]), "+v"(v[6]), "+v"(v[7]), "+v"(v[8]), "+v"(v[9]), "+v"(v[10]), "+v"(v[11]), "+v"(v[12]), "+v"(v[13]), "+v"(v[14]), "+v"(v[15]) :: "memory")
    auto finish1 = [&](unsigned r, unsigned ln, const u32x4 (&v)[NCH], const u32x2& xa, const u32x2& xb) {
      const unsigned db = 2 * r + (wt >> 2);
      const size_t off = (size_t)(tb * 32 + (ln & 31)) * D_ + db * 32 + hf2 * 16 + 4 * (ln >> 5);
      f32x2 a[4] = {{lo_f(xa[0]), hi_f(xa[0])}, {lo_f(xa[1]), hi_f(xa[1])}, {lo_f(xb[0]), hi_f(xb[0])}, {lo_f(xb[1]), hi_f(xb[1])}};
      f32x2 sm[4] = {{0.f, 0.f}, {0.f, 0.f}, {0.f, 0.f}, {0.f, 0.f}};
#pragma unroll
      for (int cc = 0; cc < NCH; ++cc) {
#pragma unroll
        for (int i = 0; i < 4; ++i) sm[i] += f32x2{lo_f(v[cc][i]), hi_f(v[cc][i])};
        if ((cc & 3) == 3) SBAR(); }
      u32x2 o0 = {cvtpk(a[0][0] + sm[0][0], a[0][1] + sm[0][1]), cvtpk(a[1][0] + sm[1][0], a[1][1] + sm[1][1])};
      u32x2 o1 = {cvtpk(a[2][0] + sm[2][0], a[2][1] + sm[2][1]), cvtpk(a[3][0] + sm[3][0], a[3][1] + sm[3][1])};
      *(u32x2*)(out + off) = o0; *(u32x2*)(out + off + 8) = o1;
    };
    u32x4 v[NCH]; u32x2 xa, xb;
    reduce1(wid, lane, v, xa, xb);
    if (wid >= 6) {
      const unsigned u = (wid - 6) * 64 + lane, ln = u >> 1, g = u & 1, db = 16 + (wt >> 2);
      const unsigned short* src = part + ((size_t)(tb * 18 + db) * 2 + hf2) * 8192 + ln * 8 + g * 4;
      u32x2 v2[NCH];
#pragma unroll
      for (int cc = 0; cc < NCH; ++cc) asm volatile("global_load_dwordx2 %0, %1, off sc0 sc1" : "=v"(v2[cc]) : "v"(src + cc * 512) : "memory");
      const size_t off = (size_t)(tb * 32 + (ln & 31)) * D_ + db * 32 + hf2 * 16 + 4 * (ln >> 5) + 8 * g;
      const u32x2 xa2 = *(const u32x2*)(x + off);
      WAITV(v); WAITV(v2);
      SBAR();
      finish1(wid, lane, v, xa, xb);
      SBAR();
      f32x2 s0 = {lo_f(xa2[0]), hi_f(xa2[0])}, s1 = {lo_f(xa2[1]), hi_f(xa2[1])}, t0 = {0.f, 0.f}, t1 = {0.f, 0.f};
#pragma unroll
      for (int cc = 0; cc < NCH; ++cc) { t0 += f32x2{lo_f(v2[cc][0]), hi_f(v2[cc][0])}; t1 += f32x2{lo_f(v2[cc][1]), hi_f(v2[cc][1])}; }
      *(u32x2*)(out + off) = u32x2{cvtpk(s0[0] + t0[0], s0[1] + t0[1]), cvtpk(s1[0] + t1[0], s1[1] + t1[1])};
    } else { WAITV(v); finish1(wid, lane, v, xa, xb); }
  }
  STAMP(10);
}

extern "C" void kernel_launch(void* const* d_in, const int* in_sizes, int n_in, void* d_out, int out_size, void* d_ws, size_t ws_size, hipStream_t stream) {
  const unsigned short* x = (const unsigned short*)d_in[0];
  const char* wqkv = (const char*)d_in[1];
  const char* wo = (const char*)d_in[2];
  const char* wg = (const char*)d_in[4]; const char* wu = (const char*)d_in[5]; const char* wd = (const char*)d_in[6];
  char* ws = (char*)d_ws;
  unsigned* flags = (unsigned*)ws;
  char* halo = ws + (1u << 20);
  char* xf = ws + (4u << 20);
  char* wqp = ws + (48u << 20);
  unsigned short* part = (unsigned short*)(ws + (8u << 20));
  (void)hipFuncSetAttribute((const void*)k_main, hipFuncAttributeMaxDynamicSharedMemorySize, LDS_BYTES);
  k_ln<<<256, 64, 0, stream>>>(x, xf, flags, wqkv, wqp);
  k_main<<<256, 512, LDS_BYTES, stream>>>(xf, wqp, wg, wu, wd, wo, part, halo, flags, x, (unsigned short*)d_out);
}
```

```cpp
#include <hip/hip_runtime.h>
#include <hip/hip_bf16.h>
#include <stdint.h>
#include <utility>
#include <type_traits>

using bf16x8 = __attribute__((ext_vector_type(8))) short;
using s16x4  = __attribute__((ext_vector_type(4))) short;
using f32x16 = __attribute__((ext_vector_type(16))) float;
using f32x2  = __attribute__((ext_vector_type(2))) float;
using u32x4  = __attribute__((ext_vector_type(4))) unsigned;
using u32x2  = __attribute__((ext_vector_type(2))) unsigned;
#define LAS __attribute__((address_space(3)))
typedef LAS const char* lds_cptr;
typedef LAS char* lds_ptr;
typedef short v4i16_t __attribute__((ext_vector_type(4)));
typedef __bf16 bf16x2_t __attribute__((ext_vector_type(2)));
#define DI __device__ __forceinline__
#define SBAR() __builtin_amdgcn_sched_barrier(0)

constexpr int S_ = 2048, D_ = 576, F_ = 4608;
constexpr int NKS = 36;
constexpr int NCH = 16;
constexpr int SL = 26624;
constexpr int NSL = 6;
constexpr int LDS_BYTES = 163840;
constexpr int QP = 232;

DI unsigned cvtpk(float lo, float hi) { f32x2 v = {lo, hi}; bf16x2_t b = __builtin_convertvector(v, bf16x2_t); return __builtin_bit_cast(unsigned, b); }
DI float rbf(float f) { return __uint_as_float(cvtpk(f, 0.f) << 16); }
DI float lo_f(unsigned p) { return __uint_as_float(p << 16); }
DI float hi_f(unsigned p) { return __uint_as_float(p & 0xffff0000u); }
DI float dot2(unsigned a, unsigned b, float c) { return __builtin_amdgcn_fdot2_f32_bf16(__builtin_bit_cast(bf16x2_t, a), __builtin_bit_cast(bf16x2_t, b), c, false); }
DI s16x4 vtr(lds_cptr p) { return __builtin_bit_cast(s16x4, __builtin_amdgcn_ds_read_tr16_b64_v4i16((LAS v4i16_t*)p)); }
DI bf16x8 trfrag(lds_cptr p, int o0, int o1) { s16x4 a = vtr(p + o0); SBAR(); s16x4 b = vtr(p + o1); return __builtin_shufflevector(a, b, 0, 1, 2, 3, 4, 5, 6, 7); }
DI void glds16s(unsigned voff, const void* sbase, unsigned lds_base) {
  asm volatile("s_mov_b32 m0, %2\n\ts_nop 0\n\tglobal_load_lds_dwordx4 %0, %1" :: "v"(voff), "s"(sbase), "s"(lds_base) : "memory", "m0");
}
#define PSTORE(p, v) asm volatile("global_store_dwordx4 %0, %1, off sc0 sc1\n\ts_nop 1" :: "v"(p), "v"(v) : "memory")
#define MFMA(a, b, c) __builtin_amdgcn_mfma_f32_32x32x16_bf16((a), (b), (c), 0, 0, 0)
template <int N> DI void wait_bar() { asm volatile("s_waitcnt vmcnt(%0)\n\ts_barrier" :: "i"(N) : "memory"); }
template <int N> DI void wait_bar_l() { asm volatile("s_waitcnt vmcnt(%0) lgkmcnt(0)\n\ts_barrier" :: "i"(N) : "memory"); }
DI void bar_l() { asm volatile("s_waitcnt lgkmcnt(0)\n\ts_barrier" ::: "memory"); }
template <int C> DI lds_cptr vaddc(unsigned v) { unsigned r; if (C == 0) r = v; else asm volatile("v_add_u32_e32 %0, %2, %1" : "=v"(r) : "v"(v), "i"(C)); return (lds_cptr)(size_t)r; }
DI unsigned fresh_lane() { unsigned l; asm volatile("v_mbcnt_lo_u32_b32 %0, -1, 0\n\tv_mbcnt_hi_u32_b32 %0, -1, %0" : "=v"(l)); return l; }

#define STAMP(i)

__global__ __launch_bounds__(64) void k_ln(const unsigned short* __restrict__ x, char* __restrict__ xf, unsigned* __restrict__ flags,
                                           const char* __restrict__ wqkv, char* __restrict__ wqp,
                                           const char* __restrict__ wg, const char* __restrict__ wu, const char* __restrict__ wd, unsigned* __restrict__ sink) {
  {
    u32x2 w[18];
#pragma unroll
    for (int i = 0; i < 18; ++i) {
      const unsigned id = (blockIdx.x * 18 + i) * 64 + threadIdx.x, rowg = id >> 5, qd = id & 31, cch = rowg / 576, row = rowg - cch * 576, pt = qd / 9;
      w[i] = u32x2{0u, 0u};
      if (qd < 27) w[i] = *(const u32x2*)(wqkv + (size_t)row * 3456 + pt * 1152 + 72 * cch + (qd - 9 * pt) * 8);
    }
#pragma unroll
    for (int i = 0; i < 18; ++i) {
      const unsigned id = (blockIdx.x * 18 + i) * 64 + threadIdx.x, rowg = id >> 5, qd = id & 31;
      *(u32x2*)(wqp + (size_t)rowg * 256 + (((qd >> 3) ^ (rowg & 3)) * 64) + (qd & 7) * 8) = w[i];
    }
  }
  const int lane = threadIdx.x, j = lane & 7, t = blockIdx.x * 8 + (lane >> 3);
  if (lane == 0) { flags[blockIdx.x * 32] = 0u; if (blockIdx.x < 32) flags[(256 + blockIdx.x) * 32] = 0u; }
  const u32x4* src = (const u32x4*)(x + (size_t)t * D_) + j;
  u32x4 v[9];
#pragma unroll
  for (int i = 0; i < 9; ++i) v[i] = src[8 * i];
  float s = 0, q = 0;
#pragma unroll
  for (int i = 0; i < 9; ++i)
#pragma unroll
    for (int e = 0; e < 4; ++e) { s = dot2(v[i][e], 0x3f803f80u, s); q = dot2(v[i][e], v[i][e], q); }
#pragma unroll
  for (int o = 1; o < 8; o <<= 1) { s += __shfl_xor(s, o); q += __shfl_xor(q, o); }
  const float m = s * (1.f / D_);
  const float var = fmaxf(q * (1.f / D_) - m * m, 0.f);
  const float mb = rbf(m), vb = rbf(var);
  const float ri = 1.f / rbf(sqrtf(rbf(vb + 1e-5f)));
  const f32x2 mb2 = {mb, mb}, ri2 = {ri, ri};
  char* gdst = xf + (size_t)((t >> 5) * NKS) * 1024 + (t & 31) * 16 + (j & 1) * 512 + (j >> 1) * 1024;
#pragma unroll
  for (int i = 0; i < 9; ++i) {
    u32x4 o;
#pragma unroll
    for (int e = 0; e < 4; ++e) { f32x2 a = {lo_f(v[i][e]), hi_f(v[i][e])}; a -= mb2; const unsigned d = cvtpk(a[0], a[1]); f32x2 b = {lo_f(d), hi_f(d)}; b *= ri2; o[e] = cvtpk(b[0], b[1]); }
    *(u32x4*)(gdst + i * 4096) = o;
  }
}

template <class F, int... I> DI void for_each_int(F&& f, std::integer_sequence<int, I...>) { (f(std::integral_constant<int, I>{}), ...); }
struct Ptrs { const char* wg; const char* wu; const char* xm; const char* wq; };

DI void dma_piece(const Ptrs& P, unsigned p, unsigned hh, unsigned ldsdst) {
  const unsigned l = fresh_lane();
  if (p < 18) {
    const unsigned mat = p >= 9 ? 1u : 0u, pj = p - 9 * mat;
    const unsigned g = l + 64 * pj, row = (g * 1821u) >> 16;
    glds16s(row * 8640u + g * 16u, (mat ? P.wu : P.wg) + (size_t)hh * (16 * F_ * 2), ldsdst + p * 1024);
  } else if (p < 22) {
    glds16s(l * 16u, P.xm + (size_t)((p - 18) * NKS + hh) * 1024, ldsdst + p * 1024);
  } else {
    glds16s(l * 16u, P.wq + (size_t)hh * 4096 + (p - 22) * 1024, ldsdst + p * 1024);
  }
}

template <int FH, class F> DI void phase1(F&& tail_dma, const Ptrs& P, const unsigned w4, const unsigned l0, lds_cptr ldsb, const unsigned rdA, const unsigned rdX, const unsigned (&rdQ)[3], f32x16 (&acc)[11]) {
  constexpr int LA = 5, RING = 6;
  auto issue_odd = [&](unsigned hh, unsigned so) {
#pragma unroll
    for (int k = 0; k < 7; ++k) if (k < 6 || w4 < 2) dma_piece(P, w4 + 4 * k, hh, l0 + so);
  };
  auto issue_evenB = [&](unsigned hh, unsigned so) {
    if (w4 < 2) dma_piece(P, 20 + w4, hh, l0 + so); else { dma_piece(P, 18 + 2 * w4, hh, l0 + so); dma_piece(P, 19 + 2 * w4, hh, l0 + so); }
  };
  auto issue_evenA = [&](unsigned hh, unsigned so) {
#pragma unroll
    for (int k = 0; k < 5; ++k) dma_piece(P, w4 + 4 * k, hh, l0 + so);
  };
  if (FH) { issue_evenB(0, 0); issue_odd(1, SL); issue_evenB(2, 2 * SL); issue_odd(3, 3 * SL); issue_evenB(4, 4 * SL); }
  else { issue_evenA(0, 0); issue_evenA(2, 2 * SL); }
  bf16x8 fr[RING]; bf16x8 xq[2];
  constexpr int NGU = FH ? 8 : 10;
  lds_cptr aG = nullptr, aQ[3] = {nullptr, nullptr, nullptr};
  const unsigned rA = l0 + rdA, rX = l0 + rdX, rQ0 = l0 + rdQ[0], rQ1 = l0 + rdQ[1], rQ2 = l0 + rdQ[2];
#define SET_SLOT(slot) do { aG = vaddc<(slot) * SL>(rA); aQ[0] = vaddc<(slot) * SL>(rQ0); if (FH) { aQ[1] = vaddc<(slot) * SL>(rQ1); aQ[2] = vaddc<(slot) * SL>(rQ2); } } while (0)
#define RD(dst, n) do { constexpr int nn_ = (n) % 66, slot_ = nn_ / 11, t_ = nn_ % 11; if (t_ == 0) SET_SLOT(slot_); \
    if (t_ < NGU) dst = trfrag(aG, (t_ & 1) * 9216 + (t_ >> 1) * 64, (t_ & 1) * 9216 + (t_ >> 1) * 64 + 4 * 576); else dst = trfrag(aQ[t_ < NGU ? 0 : t_ - NGU], 0, 4 * 256); } while (0)
#define RDX(slot) (*(const LAS bf16x8*)vaddc<(slot) * SL>(rX))
  if (FH) { if (w4 < 2) wait_bar<9>(); else wait_bar<10>(); } else wait_bar<5>();
  STAMP(1);
  xq[0] = RDX(0);
  SBAR();
  RD(fr[0], 0); SBAR(); RD(fr[1], 1); SBAR(); RD(fr[2], 2); SBAR(); RD(fr[3], 3); SBAR(); RD(fr[4], 4); SBAR();
#pragma unroll 1
  for (int it = 0; it < 6; ++it) {
    auto step = [&](auto n_) {
      constexpr int n = decltype(n_)::value;
      constexpr int S3 = n / 22, m = n % 22;
      const int st = 3 * it + S3;
      if (m == 22 - LA) {
        if (!FH && st <= 15) issue_evenA(2 * st + 4, ((2 * S3 + 4) % 6) * SL);
        if (st < 17) {
          if (st == 16) wait_bar<0>();
          else if (FH) { if (w4 < 2) wait_bar<1>(); else wait_bar<2>(); }
          else wait_bar<5>();
        }
        xq[0] = RDX((2 * S3 + 2) % 6);
        if (FH) { if (st <= 15) issue_odd(2 * st + 5, ((2 * S3 + 5) % 6) * SL); if (st <= 14) issue_evenB(2 * st + 6, ((2 * S3 + 6) % 6) * SL); }
        if (S3 == 1 && st == 16) tail_dma();
      }
      if (m == 11 - LA) xq[1] = RDX(2 * S3 + 1);
      const bf16x8 xcur = xq[m / 11];
      RD(fr[(n + LA) % RING], n + LA);
      acc[m % 11] = MFMA(fr[n % RING], xcur, acc[m % 11]);
      SBAR();
    };
    for_each_int(step, std::make_integer_sequence<int, 66>{});
  }
#undef RD
#undef RDX
#undef SET_SLOT
}

__global__ __launch_bounds__(512, 2) void k_main(const char* __restrict__ xf, const char* __restrict__ wqkv, const char* __restrict__ wg, const char* __restrict__ wu,
                                                 const char* __restrict__ wd, const char* __restrict__ wo, unsigned short* __restrict__ part,
                                                 char* __restrict__ halo, unsigned* __restrict__ flags,
                                                 const unsigned short* __restrict__ x, unsigned short* __restrict__ out) {
  extern __shared__ __attribute__((aligned(16))) char lds[];
  STAMP(0);
  const int tid = threadIdx.x, lane = tid & 63;
  const unsigned wid = __builtin_amdgcn_readfirstlane(tid >> 6);
  const unsigned tg = wid & 3, fh = wid >> 2;
  const unsigned bx = blockIdx.x, xcd = bx & 7, kk = bx >> 3;
  const unsigned c = xcd * 2 + (kk & 1), mt = kk >> 1;
  const unsigned l0 = (unsigned)(uintptr_t)lds;
  const lds_cptr ldsb = (lds_cptr)lds;
  Ptrs P;
  P.wg = wg + 576 * c; P.wu = wu + 576 * c; P.xm = xf + (size_t)(mt * 4) * NKS * 1024; P.wq = wqkv + (size_t)c * (576 * 256);
  const unsigned h = lane >> 5, blk16 = (lane >> 4) & 1, q4 = (lane & 15) >> 2, p4 = lane & 3;
  const unsigned rdA = (8 * h + q4) * 576 + blk16 * 32 + p4 * 8 + fh * 5 * 64;
  const unsigned rdX = 18432 + tg * 1024 + lane * 16;
  unsigned rdQ[3];
#pragma unroll
  for (int i = 0; i < 3; ++i) { const unsigned t = fh ? i + 1 : 0; rdQ[i] = 22528 + (8 * h + q4) * 256 + ((t ^ q4) * 64) + blk16 * 32 + p4 * 8; }
  const unsigned lrow = lane >> 3, ch2 = (lane & 7) ^ (((lrow >> 1) & 1) << 2);
  const unsigned voff2 = lrow * (D_ * 2) + ch2 * 16;
  auto dma2j = [&](int q, unsigned j) {
    const int hh = (2 * q) % 6;
    const unsigned so = j >= 22 ? ((hh + 1) % 6) * SL + (j - 22) * 1024 : hh * SL + j * 1024;
    if (j < 36) glds16s(voff2, wd + (size_t)(288 * c + 8 * j) * (D_ * 2) + q * 128, l0 + so);
    else { const unsigned l = fresh_lane(), lr = l >> 3; unsigned wr = 36 * c + 8 * (j - 36) + lr; if (wr > 575) wr = 575;
      glds16s(wr * (D_ * 2) + (((l & 7) ^ (((lr >> 1) & 1) << 2)) * 16), wo + q * 128, l0 + so); }
  };
  auto dma2_A = [&](int q) {
#pragma unroll
    for (int k = 0; k < 5; ++k) dma2j(q, tg + 4 * k);
  };
  auto dma2_Bodd = [&](int q) {
#pragma unroll
    for (int k = 0; k < 5; ++k) dma2j(q, 22 + tg + 4 * k);
  };
  auto dma2_Beven = [&](int q) { if (tg < 2) dma2j(q, 20 + tg); };
  auto dma2_all = [&](int q) { if (fh) { dma2_Beven(q); dma2_Bodd(q); } else dma2_A(q); };
  const f32x16 zero = {};
  f32x16 acc[11];
#pragma unroll
  for (int i = 0; i < 11; ++i) acc[i] = zero;
  if (fh) phase1<1>([&]() { dma2_all(0); }, P, tg, l0, ldsb, rdA, rdX, rdQ, acc); else phase1<0>([&]() { dma2_all(0); }, P, tg, l0, ldsb, rdA, rdX, rdQ, acc);
  u32x4 own[10];
#pragma unroll
  for (int b = 0; b < 5; ++b) if (b < 4 || fh == 0)
#pragma unroll
    for (int i = 0; i < 8; ++i) {
      const unsigned gp = cvtpk(fmaxf(acc[2 * b][2 * i], 0.f), fmaxf(acc[2 * b][2 * i + 1], 0.f)), up = cvtpk(acc[2 * b + 1][2 * i], acc[2 * b + 1][2 * i + 1]);
      own[2 * b + (i >> 2)][i & 3] = cvtpk(lo_f(gp) * lo_f(up), hi_f(gp) * hi_f(up));
    }
  if (wid == 7) {
    char* hb = halo + (size_t)(mt * 16 + c) * 4096 + ((lane & 15) * 144);
    if (lane & 16) {
#pragma unroll
      for (int t = 1; t < 4; ++t)
#pragma unroll
        for (int g = 0; g < 4; ++g) {
          const int c0 = 32 * t + 8 * g;
          if (c0 + 4 >= 36 && c0 < 108) {
            const u32x2 w = {cvtpk(acc[7 + t][4 * g], acc[7 + t][4 * g + 1]), cvtpk(acc[7 + t][4 * g + 2], acc[7 + t][4 * g + 3])};
            const int cc = c0 + 4 * (int)h;
            if (cc >= 36 && cc < 108) asm volatile("global_store_dwordx2 %0, %1, off sc0 sc1" :: "v"(hb + (cc - 36) * 2), "v"(w) : "memory");
          }
        }
    }
  }
  bar_l();
  STAMP(2);
  if (wid == 7) {
    asm volatile("s_waitcnt vmcnt(0)" ::: "memory");
    if (lane == 0) __hip_atomic_store(flags + (mt * 16 + c) * 32, 1u, __ATOMIC_RELAXED, __HIP_MEMORY_SCOPE_AGENT);
  }
  constexpr int HX = 2 * SL, QK = HX + 73728;
  const unsigned hxo = HX + tg * 18432 + lane * 16;
  {
    const lds_ptr hx = (lds_ptr)lds + hxo + fh * 10240;
#pragma unroll
    for (int i = 0; i < 10; ++i) if (i < 8 || fh == 0) *(LAS u32x4*)(hx + i * 1024) = own[i];
    lds_ptr d = (lds_ptr)lds + QK + (16 + tg * 32 + (lane & 31)) * QP + 8 * h;
    auto put = [&](const f32x16& a, int t) {
#pragma unroll
      for (int g = 0; g < 4; ++g) if (32 * t + 8 * g < 108) {
        const u32x2 w = {cvtpk(a[4 * g], a[4 * g + 1]), cvtpk(a[4 * g + 2], a[4 * g + 3])};
        if (32 * t + 8 * g + 4 < 108 || h == 0) *(LAS u32x2*)(d + (32 * t + 8 * g) * 2) = w;
      }
    };
    if (fh) { put(acc[8], 1); put(acc[9], 2); put(acc[10], 3); } else put(acc[10], 0);
  }
  if (mt > 0 && wid == 0) {
    const unsigned* f = flags + ((mt - 1) * 16 + c) * 32;
    unsigned spins = 0;
    while (__builtin_amdgcn_readfirstlane(__hip_atomic_load(f, __ATOMIC_RELAXED, __HIP_MEMORY_SCOPE_AGENT)) == 0u && ++spins < (1u << 22)) __builtin_amdgcn_s_sleep(1);
    asm volatile("" ::: "memory");
    const char* hb = halo + (size_t)((mt - 1) * 16 + c) * 4096;
    u32x4 v[3];
#pragma unroll
    for (int i = 0; i < 3; ++i) { unsigned id = lane + 64 * i; if (id > 143) id = 143; asm volatile("global_load_dwordx4 %0, %1, off sc0 sc1" : "=v"(v[i]) : "v"(hb + id * 16) : "memory"); }
    asm volatile("s_waitcnt vmcnt(0)" : "+v"(v[0]), "+v"(v[1]), "+v"(v[2]) :: "memory");
#pragma unroll
    for (int i = 0; i < 3; ++i) {
      const unsigned id = lane + 64 * i;
      if (id < 144) {
        const unsigned tk = id / 9, ck = id - tk * 9;
        lds_ptr d = (lds_ptr)lds + QK + tk * QP + 72 + ck * 16;
        *(LAS u32x2*)d = u32x2{v[i][0], v[i][1]}; *(LAS u32x2*)(d + 8) = u32x2{v[i][2], v[i][3]};
      }
    }
  }
  if (mt == 0 && wid == 0) {
#pragma unroll
    for (int i = 0; i < 8; ++i) { const unsigned id = lane + 64 * i; if (id < 16 * (QP / 8)) *(LAS u32x2*)((lds_ptr)lds + QK + id * 8) = u32x2{0u, 0u}; }
  }
  bar_l();
  STAMP(3);
  bf16x8 hf[21];
#pragma unroll
  for (int s = 0; s < 18; ++s) hf[s] = *(const LAS bf16x8*)(ldsb + hxo + s * 1024);
  STAMP(5);
  float r10[10];
  const int tok = (tid & 31) + 32 * ((tid >> 6) & 3), wp = (tid >> 5) & 1; const int head = tid >> 8;
  {
    const int t = 128 * mt + tok, slot = 16 + tok;
    const int ox = head * 40, oy = head ? 36 : 32;
    const lds_cptr qp = ldsb + QK + slot * QP;
    unsigned qv[9];
#pragma unroll
    for (int i = 0; i < 4; ++i) { const u32x2 u = *(const LAS u32x2*)(qp + ox + 8 * i); qv[2 * i] = u[0]; qv[2 * i + 1] = u[1]; }
    qv[8] = *(const LAS unsigned*)(qp + oy);
    float lg[4];
#pragma unroll
    for (int i4 = 0; i4 < 4; ++i4) {
      const int w = 4 * wp + i4;
      const lds_cptr kp = qp - 2 * w * QP + 72;
      float a = 0;
#pragma unroll
      for (int i = 0; i < 4; ++i) { const u32x2 u = *(const LAS u32x2*)(kp + ox + 8 * i); a = dot2(qv[2 * i], u[0], a); a = dot2(qv[2 * i + 1], u[1], a); }
      a = dot2(qv[8], *(const LAS unsigned*)(kp + oy), a);
      lg[i4] = (t - 2 * w >= 0) ? rbf(rbf(a) * 0.23570226039551584f) : -1e9f;
    }
    float mx = fmaxf(fmaxf(lg[0], lg[1]), fmaxf(lg[2], lg[3]));
    mx = fmaxf(mx, __shfl_xor(mx, 32));
    float sum = 0;
#pragma unroll
    for (int i = 0; i < 4; ++i) { lg[i] = __expf(lg[i] - mx); sum += lg[i]; }
    sum += __shfl_xor(sum, 32);
    const float inv = 1.f / sum;
    f32x2 o[9];
#pragma unroll
    for (int i = 0; i < 9; ++i) o[i] = f32x2{0.f, 0.f};
#pragma unroll
    for (int i4 = 0; i4 < 4; ++i4) {
      const int w = 4 * wp + i4;
      const float p = rbf(lg[i4] * inv); const f32x2 p2 = {p, p};
      const lds_cptr vp = qp - 2 * w * QP + 144;
#pragma unroll
      for (int i = 0; i < 4; ++i) { const u32x2 u = *(const LAS u32x2*)(vp + ox + 8 * i);
        o[2 * i] += p2 * f32x2{lo_f(u[0]), hi_f(u[0])}; o[2 * i + 1] += p2 * f32x2{lo_f(u[1]), hi_f(u[1])}; }
      const unsigned y = *(const LAS unsigned*)(vp + oy);
      o[8] += p2 * f32x2{lo_f(y), hi_f(y)};
    }
#pragma unroll
    for (int i = 0; i < 5; ++i) {
      f32x2 a = (i < 4) ? o[i] : o[8], b = (i < 4) ? o[4 + i] : f32x2{0.f, 0.f};
      asm volatile("" : "+v"(a), "+v"(b));
      const f32x2 send = wp ? a : b, keep = wp ? b : a;
      r10[2 * i] = keep[0] + __shfl_xor(send[0], 32); r10[2 * i + 1] = keep[1] + __shfl_xor(send[1], 32);
    }
  }
  bar_l();
  {
    lds_ptr op = (lds_ptr)lds + HX + tok * 104 + head * 36;
#pragma unroll
    for (int i = 0; i < 4; ++i) *(LAS unsigned*)(op + 4 * (head + 4 * wp + i)) = cvtpk(r10[2 * i], r10[2 * i + 1]);
    if (wp == 0) *(LAS unsigned*)(op + (head ? 0 : 32)) = cvtpk(r10[8], r10[9]);
    if (head == 1 && wp == 1) { lds_ptr z = (lds_ptr)lds + HX + tok * 104 + 72; *(LAS u32x2*)z = u32x2{0u, 0u}; *(LAS u32x2*)(z + 8) = u32x2{0u, 0u}; *(LAS u32x2*)(z + 16) = u32x2{0u, 0u}; }
  }
  bar_l();
  STAMP(6);
#pragma unroll
  for (int s = 0; s < 3; ++s) {
    const lds_cptr p = ldsb + HX + (tg * 32 + (lane & 31)) * 104 + (16 * s + 4 * h) * 2;
    const u32x2 a = *(const LAS u32x2*)p, b = *(const LAS u32x2*)(p + 16);
    hf[18 + s] = __builtin_bit_cast(bf16x8, u32x4{a[0], a[1], b[0], b[1]});
  }
  wait_bar_l<0>();
  dma2_all(1); dma2_all(2);
  STAMP(7);
  const unsigned colb = (((4 * fh + 2 * blk16 + (p4 >> 1)) ^ ((q4 >> 1) << 2)) * 16) + 8 * (p4 & 1);
  const lds_cptr rb2 = ldsb + (4 * h + q4) * 128 + colb;
  unsigned short* pp = part + (((size_t)(mt * 4 + tg) * 18 + fh) * 32 + c) * 512 + lane * 8;
  {
    constexpr int LA = 6, RING = 7, CNT = 21;
    bf16x8 fr[RING];
    auto rd2 = [&](int q, int n) -> bf16x8 {
      const int hh = (2 * q + (n >= 11 ? 1 : 0)) % 6, nn = n >= 11 ? n - 11 : n;
      return trfrag(rb2 + hh * SL, nn * 2048, nn * 2048 + 1024);
    };
#pragma unroll
    for (int n = 0; n < LA; ++n) { fr[n] = rd2(0, n); SBAR(); }
    f32x16 acc2[2];
#pragma unroll
    for (int q = 0; q < 9; ++q) {
#pragma unroll
      for (int n = 0; n < CNT; ++n) {
        if (n == CNT - LA && q < 8) {
          if (!fh && q >= 1 && q + 2 < 9) dma2_A(q + 2);
          if (fh) {
            if (q == 0) { if (tg < 2) wait_bar<6>(); else wait_bar<5>(); }
            else if (q < 7) { if (tg < 2) wait_bar<3>(); else wait_bar<2>(); }
            else wait_bar<2>();
          } else {
            if (q == 0) wait_bar<5>(); else if (q < 7) wait_bar<7>(); else wait_bar<2>();
          }
          if (fh) { if (q >= 1 && q + 2 < 9) dma2_Bodd(q + 2); if (q + 3 < 9) dma2_Beven(q + 3); }
          if (q == 7 && tid == 64) __hip_atomic_fetch_add(flags + (256 + mt) * 32, 1u, __ATOMIC_RELAXED, __HIP_MEMORY_SCOPE_AGENT);
        }
        if (n + LA < CNT) fr[(n + LA) % RING] = rd2(q, n + LA);
        else if (q < 8) fr[(n + LA) % RING] = rd2(q + 1, n + LA - CNT);
        acc2[q & 1] = MFMA(fr[n % RING], hf[n], n == 0 ? zero : acc2[q & 1]);
        if (n == 3 && q > 0) {
          u32x4 o0, o1;
#pragma unroll
          for (int i = 0; i < 4; ++i) { o0[i] = cvtpk(acc2[(q - 1) & 1][2 * i], acc2[(q - 1) & 1][2 * i + 1]); o1[i] = cvtpk(acc2[(q - 1) & 1][8 + 2 * i], acc2[(q - 1) & 1][8 + 2 * i + 1]); }
          PSTORE(pp + (q - 1) * 32768, o0); PSTORE(pp + (q - 1) * 32768 + 8192, o1);
        }
        SBAR();
      }
    }
    u32x4 o0, o1;
#pragma unroll
    for (int i = 0; i < 4; ++i) { o0[i] = cvtpk(acc2[0][2 * i], acc2[0][2 * i + 1]); o1[i] = cvtpk(acc2[0][8 + 2 * i], acc2[0][8 + 2 * i + 1]); }
    PSTORE(pp + 8 * 32768, o0); PSTORE(pp + 8 * 32768 + 8192, o1);
  }
  STAMP(8);
  asm volatile("s_waitcnt vmcnt(0)" ::: "memory");
  __builtin_amdgcn_s_barrier();
  if (tid == 0) __hip_atomic_fetch_add(flags + (272 + mt) * 32, 1u, __ATOMIC_RELAXED, __HIP_MEMORY_SCOPE_AGENT);
  {
    const unsigned* cn = flags + ((wid < 6 ? 256 : 272) + mt) * 32;
    unsigned spins = 0;
    while (__builtin_amdgcn_readfirstlane(__hip_atomic_load(cn, __ATOMIC_RELAXED, __HIP_MEMORY_SCOPE_AGENT)) < 16u && ++spins < (1u << 22)) __builtin_amdgcn_s_sleep(1);
    asm volatile("" ::: "memory");
  }
  STAMP(9);
  {
    const unsigned wt = c >> 1, tb = mt * 4 + (wt & 3), hf2 = c & 1;
    auto reduce1 = [&](unsigned r, unsigned ln, u32x4 (&v)[NCH], u32x2& xa, u32x2& xb) {
      const unsigned db = 2 * r + (wt >> 2);
      const unsigned short* src = part + ((size_t)(tb * 18 + db) * 2 + hf2) * 8192 + ln * 8;
#pragma unroll
      for (int cc = 0; cc < NCH; ++cc) asm volatile("global_load_dwordx4 %0, %1, off sc0 sc1" : "=v"(v[cc]) : "v"(src + cc * 512) : "memory");
      const size_t off = (size_t)(tb * 32 + (ln & 31)) * D_ + db * 32 + hf2 * 16 + 4 * (ln >> 5);
      xa = *(const u32x2*)(x + off); xb = *(const u32x2*)(x + off + 8);
    };
#define WAITV(v) asm volatile("s_waitcnt vmcnt(0)" : "+v"(v[0]), "+v"(v[1]), "+v"(v[2]), "+v"(v[3]), "+v"(v[4]), "+v"(v[5]), "+v"(v[6]), "+v"(v[7]), "+v"(v[8]), "+v"(v[9]), "+v"(v[10]), "+v"(v[11]), "+v"(v[12]), "+v"(v[13]), "+v"(v[14]), "+v"(v[15]) :: "memory")
    auto finish1 = [&](unsigned r, unsigned ln, const u32x4 (&v)[NCH], const u32x2& xa, const u32x2& xb) {
      const unsigned db = 2 * r + (wt >> 2);
      const size_t off = (size_t)(tb * 32 + (ln & 31)) * D_ + db * 32 + hf2 * 16 + 4 * (ln >> 5);
      f32x2 a[4] = {{lo_f(xa[0]), hi_f(xa[0])}, {lo_f(xa[1]), hi_f(xa[1])}, {lo_f(xb[0]), hi_f(xb[0])}, {lo_f(xb[1]), hi_f(xb[1])}};
      f32x2 sm[4] = {{0.f, 0.f}, {0.f, 0.f}, {0.f, 0.f}, {0.f, 0.f}};
#pragma unroll
      for (int cc = 0; cc < NCH; ++cc) {
#pragma unroll
        for (int i = 0; i < 4; ++i) sm[i] += f32x2{lo_f(v[cc][i]), hi_f(v[cc][i])};
        if ((cc & 3) == 3) SBAR(); }
      u32x2 o0 = {cvtpk(a[0][0] + sm[0][0], a[0][1] + sm[0][1]), cvtpk(a[1][0] + sm[1][0], a[1][1] + sm[1][1])};
      u32x2 o1 = {cvtpk(a[2][0] + sm[2][0], a[2][1] + sm[2][1]), cvtpk(a[3][0] + sm[3][0], a[3][1] + sm[3][1])};
      *(u32x2*)(out + off) = o0; *(u32x2*)(out + off + 8) = o1;
    };
    u32x4 v[NCH]; u32x2 xa, xb;
    reduce1(wid, lane, v, xa, xb);
    if (wid >= 6) {
      const unsigned u = (wid - 6) * 64 + lane, ln = u >> 1, g = u & 1, db = 16 + (wt >> 2);
      const unsigned short* src = part + ((size_t)(tb * 18 + db) * 2 + hf2) * 8192 + ln * 8 + g * 4;
      u32x2 v2[NCH];
#pragma unroll
      for (int cc = 0; cc < NCH; ++cc) asm volatile("global_load_dwordx2 %0, %1, off sc0 sc1" : "=v"(v2[cc]) : "v"(src + cc * 512) : "memory");
      const size_t off = (size_t)(tb * 32 + (ln & 31)) * D_ + db * 32 + hf2 * 16 + 4 * (ln >> 5) + 8 * g;
      const u32x2 xa2 = *(const u32x2*)(x + off);
      WAITV(v); WAITV(v2);
      SBAR();
      finish1(wid, lane, v, xa, xb);
      SBAR();
      f32x2 s0 = {lo_f(xa2[0]), hi_f(xa2[0])}, s1 = {lo_f(xa2[1]), hi_f(xa2[1])}, t0 = {0.f, 0.f}, t1 = {0.f, 0.f};
#pragma unroll
      for (int cc = 0; cc < NCH; ++cc) { t0 += f32x2{lo_f(v2[cc][0]), hi_f(v2[cc][0])}; t1 += f32x2{lo_f(v2[cc][1]), hi_f(v2[cc][1])}; }
      *(u32x2*)(out + off) = u32x2{cvtpk(s0[0] + t0[0], s0[1] + t0[1]), cvtpk(s1[0] + t1[0], s1[1] + t1[1])};
    } else { WAITV(v); finish1(wid, lane, v, xa, xb); }
  }
  STAMP(10);
}

extern "C" void kernel_launch(void* const* d_in, const int* in_sizes, int n_in, void* d_out, int out_size, void* d_ws, size_t ws_size, hipStream_t stream) {
  const unsigned short* x = (const unsigned short*)d_in[0];
  const char* wqkv = (const char*)d_in[1];
  const char* wo = (const char*)d_in[2];
  const char* wg = (const char*)d_in[4]; const char* wu = (const char*)d_in[5]; const char* wd = (const char*)d_in[6];
  char* ws = (char*)d_ws;
  unsigned* flags = (unsigned*)ws;
  char* halo = ws + (1u << 20);
  char* xf = ws + (4u << 20);
  char* wqp = ws + (48u << 20);
  unsigned short* part = (unsigned short*)(ws + (8u << 20));
  (void)hipFuncSetAttribute((const void*)k_main, hipFuncAttributeMaxDynamicSharedMemorySize, LDS_BYTES);
  k_ln<<<256, 64, 0, stream>>>(x, xf, flags, wqkv, wqp, wg, wu, wd, (unsigned*)(ws + (2u << 20)));
  k_main<<<256, 512, LDS_BYTES, stream>>>(xf, wqp, wg, wu, wd, wo, part, halo, flags, x, (unsigned short*)d_out);
}
```

```cpp
#include <hip/hip_runtime.h>
#include <hip/hip_bf16.h>
#include <stdint.h>
#include <utility>
#include <type_traits>

using bf16x8 = __attribute__((ext_vector_type(8))) short;
using s16x4  = __attribute__((ext_vector_type(4))) short;
using f32x16 = __attribute__((ext_vector_type(16))) float;
using f32x2  = __attribute__((ext_vector_type(2))) float;
using u32x4  = __attribute__((ext_vector_type(4))) unsigned;
using u32x2  = __attribute__((ext_vector_type(2))) unsigned;
#define LAS __attribute__((address_space(3)))
typedef LAS const char* lds_cptr;
typedef LAS char* lds_ptr;
typedef short v4i16_t __attribute__((ext_vector_type(4)));
typedef __bf16 bf16x2_t __attribute__((ext_vector_type(2)));
#define DI __device__ __forceinline__
#define SBAR() __builtin_amdgcn_sched_barrier(0)

constexpr int S_ = 2048, D_ = 576, F_ = 4608;
constexpr int NKS = 36;
constexpr int NCH = 16;
constexpr int SL = 26624;
constexpr int NSL = 6;
constexpr int LDS_BYTES = 163840;
constexpr int QP = 232;

DI unsigned cvtpk(float lo, float hi) { f32x2 v = {lo, hi}; bf16x2_t b = __builtin_convertvector(v, bf16x2_t); return __builtin_bit_cast(unsigned, b); }
DI float rbf(float f) { return __uint_as_float(cvtpk(f, 0.f) << 16); }
DI float lo_f(unsigned p) { return __uint_as_float(p << 16); }
DI float hi_f(unsigned p) { return __uint_as_float(p & 0xffff0000u); }
DI float dot2(unsigned a, unsigned b, float c) { return __builtin_amdgcn_fdot2_f32_bf16(__builtin_bit_cast(bf16x2_t, a), __builtin_bit_cast(bf16x2_t, b), c, false); }
DI s16x4 vtr(lds_cptr p) { return __builtin_bit_cast(s16x4, __builtin_amdgcn_ds_read_tr16_b64_v4i16((LAS v4i16_t*)p)); }
DI bf16x8 trfrag(lds_cptr p, int o0, int o1) { s16x4 a = vtr(p + o0); SBAR(); s16x4 b = vtr(p + o1); return __builtin_shufflevector(a, b, 0, 1, 2, 3, 4, 5, 6, 7); }
DI void glds16s(unsigned voff, const void* sbase, unsigned lds_base) {
  asm volatile("s_mov_b32 m0, %2\n\ts_nop 0\n\tglobal_load_lds_dwordx4 %0, %1" :: "v"(voff), "s"(sbase), "s"(lds_base) : "memory", "m0");
}
#define PSTORE(p, v) asm volatile("global_store_dwordx4 %0, %1, off sc0 sc1\n\ts_nop 1" :: "v"(p), "v"(v) : "memory")
#define MFMA(a, b, c) __builtin_amdgcn_mfma_f32_32x32x16_bf16((a), (b), (c), 0, 0, 0)
template <int N> DI void wait_bar() { asm volatile("s_waitcnt vmcnt(%0)\n\ts_barrier" :: "i"(N) : "memory"); }
template <int N> DI void wait_bar_l() { asm volatile("s_waitcnt vmcnt(%0) lgkmcnt(0)\n\ts_barrier" :: "i"(N) : "memory"); }
DI void bar_l() { asm volatile("s_waitcnt lgkmcnt(0)\n\ts_barrier" ::: "memory"); }
template <int C> DI lds_cptr vaddc(unsigned v) { unsigned r; if (C == 0) r = v; else asm volatile("v_add_u32_e32 %0, %2, %1" : "=v"(r) : "v"(v), "i"(C)); return (lds_cptr)(size_t)r; }
DI unsigned fresh_lane() { unsigned l; asm volatile("v_mbcnt_lo_u32_b32 %0, -1, 0\n\tv_mbcnt_hi_u32_b32 %0, -1, %0" : "=v"(l)); return l; }

#define STAMP(i)

typedef unsigned long long u64;
#define FL(i) (fl64 + (size_t)(i) * 16)
constexpr int FL_LN = 1, FL_HALO = 257, FL_A = 513, FL_B = 769;
DI u64 fl_load(const u64* p) { return __hip_atomic_load(p, __ATOMIC_RELAXED, __HIP_MEMORY_SCOPE_AGENT); }
DI void fl_store(u64* p, u64 v) { __hip_atomic_store(p, v, __ATOMIC_RELAXED, __HIP_MEMORY_SCOPE_AGENT); }
DI void fl_wait(const u64* p, u64 tag) { unsigned spins = 0; while (!__all(fl_load(p) == tag) && ++spins < (1u << 22)) __builtin_amdgcn_s_sleep(1); asm volatile("" ::: "memory"); }
#define ST128(p, v) asm volatile("global_store_dwordx4 %0, %1, off sc0 sc1\n\ts_nop 1" :: "v"(p), "v"(v) : "memory")
#define ST64(p, v) asm volatile("global_store_dwordx2 %0, %1, off sc0 sc1" :: "v"(p), "v"(v) : "memory")

template <class F, int... I> DI void for_each_int(F&& f, std::integer_sequence<int, I...>) { (f(std::integral_constant<int, I>{}), ...); }
#define PFST 4
#define NPF 1
struct Ptrs { const char* wg; const char* wu; const char* xm; const char* wq; };

DI void dma_piece(const Ptrs& P, unsigned p, unsigned hh, unsigned ldsdst) {
  const unsigned l = fresh_lane();
  if (p < 18) {
    const unsigned mat = p >= 9 ? 1u : 0u, pj = p - 9 * mat;
    const unsigned g = l + 64 * pj, row = (g * 1821u) >> 16;
    glds16s(row * 8640u + g * 16u, (mat ? P.wu : P.wg) + (size_t)hh * (16 * F_ * 2), ldsdst + p * 1024);
  } else if (p < 22) {
    glds16s(l * 16u, P.xm + (size_t)((p - 18) * NKS + hh) * 1024, ldsdst + p * 1024);
  } else {
    glds16s(l * 16u, P.wq + (size_t)hh * 4096 + (p - 22) * 1024, ldsdst + p * 1024);
  }
}

template <int FH, class F, class G> DI void phase1(F&& tail_dma, G&& l2_prefetch, const Ptrs& P, const unsigned w4, const unsigned l0, lds_cptr ldsb, const unsigned rdA, const unsigned rdX, const unsigned (&rdQ)[3], f32x16 (&acc)[11]) {
  constexpr int LA = 5, RING = 6;
  auto issue_odd = [&](unsigned hh, unsigned so) {
#pragma unroll
    for (int k = 0; k < 7; ++k) if (k < 6 || w4 < 2) dma_piece(P, w4 + 4 * k, hh, l0 + so);
  };
  auto issue_evenB = [&](unsigned hh, unsigned so) {
    if (w4 < 2) dma_piece(P, 20 + w4, hh, l0 + so); else { dma_piece(P, 18 + 2 * w4, hh, l0 + so); dma_piece(P, 19 + 2 * w4, hh, l0 + so); }
  };
  auto issue_evenA = [&](unsigned hh, unsigned so) {
#pragma unroll
    for (int k = 0; k < 5; ++k) dma_piece(P, w4 + 4 * k, hh, l0 + so);
  };
  bf16x8 fr[RING]; bf16x8 xq[2];
  constexpr int NGU = FH ? 8 : 10;
  lds_cptr aG = nullptr, aQ[3] = {nullptr, nullptr, nullptr};
  const unsigned rA = l0 + rdA, rX = l0 + rdX, rQ0 = l0 + rdQ[0], rQ1 = l0 + rdQ[1], rQ2 = l0 + rdQ[2];
#define SET_SLOT(slot) do { aG = vaddc<(slot) * SL>(rA); aQ[0] = vaddc<(slot) * SL>(rQ0); if (FH) { aQ[1] = vaddc<(slot) * SL>(rQ1); aQ[2] = vaddc<(slot) * SL>(rQ2); } } while (0)
#define RD(dst, n) do { constexpr int nn_ = (n) % 66, slot_ = nn_ / 11, t_ = nn_ % 11; if (t_ == 0) SET_SLOT(slot_); \
    if (t_ < NGU) dst = trfrag(aG, (t_ & 1) * 9216 + (t_ >> 1) * 64, (t_ & 1) * 9216 + (t_ >> 1) * 64 + 4 * 576); else dst = trfrag(aQ[t_ < NGU ? 0 : t_ - NGU], 0, 4 * 256); } while (0)
#define RDX(slot) (*(const LAS bf16x8*)vaddc<(slot) * SL>(rX))
  xq[0] = RDX(0);
  SBAR();
  RD(fr[0], 0); SBAR(); RD(fr[1], 1); SBAR(); RD(fr[2], 2); SBAR(); RD(fr[3], 3); SBAR(); RD(fr[4], 4); SBAR();
#pragma unroll 1
  for (int it = 0; it < 6; ++it) {
    auto step = [&](auto n_) {
      constexpr int n = decltype(n_)::value;
      constexpr int S3 = n / 22, m = n % 22;
      const int st = 3 * it + S3;
      if (m == 22 - LA) {
        if (!FH && st <= 15) issue_evenA(2 * st + 4, ((2 * S3 + 4) % 6) * SL);
        if (st < 17) {
          if (st == 16) wait_bar<0>();
          else if (FH) { if (st == PFST + 1) { if (w4 < 2) wait_bar<1 + NPF>(); else wait_bar<2 + NPF>(); } else { if (w4 < 2) wait_bar<1>(); else wait_bar<2>(); } }
          else wait_bar<5>();
        }
        xq[0] = RDX((2 * S3 + 2) % 6);
        if (FH) { if (st <= 15) issue_odd(2 * st + 5, ((2 * S3 + 5) % 6) * SL); if (st <= 14) issue_evenB(2 * st + 6, ((2 * S3 + 6) % 6) * SL); }
        if (S3 == 1 && st == 16) tail_dma();
        if (FH && st == PFST) l2_prefetch();
      }
      if (m == 11 - LA) xq[1] = RDX(2 * S3 + 1);
      const bf16x8 xcur = xq[m / 11];
      RD(fr[(n + LA) % RING], n + LA);
      acc[m % 11] = MFMA(fr[n % RING], xcur, acc[m % 11]);
      SBAR();
    };
    for_each_int(step, std::make_integer_sequence<int, 66>{});
  }
#undef RD
#undef RDX
#undef SET_SLOT
}

__global__ __launch_bounds__(512, 2) void k_main(const unsigned short* __restrict__ x, const char* __restrict__ wqkv, const char* __restrict__ wg, const char* __restrict__ wu,
                                                 const char* __restrict__ wd, const char* __restrict__ wo, unsigned short* __restrict__ part,
                                                 char* __restrict__ halo, u64* __restrict__ fl64, char* __restrict__ xf, char* __restrict__ wqp,
                                                 unsigned short* __restrict__ out) {
  extern __shared__ __attribute__((aligned(16))) char lds[];
  STAMP(0);
  const int tid = threadIdx.x, lane = tid & 63;
  const unsigned wid = __builtin_amdgcn_readfirstlane(tid >> 6);
  const unsigned tg = wid & 3, fh = wid >> 2;
  const unsigned bx = blockIdx.x, xcd = bx & 7, kk = bx >> 3;
  const unsigned c = xcd * 2 + (kk & 1), mt = kk >> 1;
  const unsigned l0 = (unsigned)(uintptr_t)lds;
  const lds_cptr ldsb = (lds_cptr)lds;
  Ptrs P;
  P.wg = wg + 576 * c; P.wu = wu + 576 * c; P.xm = xf + (size_t)(mt * 4) * NKS * 1024; P.wq = wqp + (size_t)c * (576 * 256);
  const unsigned h = lane >> 5, blk16 = (lane >> 4) & 1, q4 = (lane & 15) >> 2, p4 = lane & 3;
  const unsigned rdA = (8 * h + q4) * 576 + blk16 * 32 + p4 * 8 + fh * 5 * 64;
  const unsigned rdX = 18432 + tg * 1024 + lane * 16;
  unsigned rdQ[3];
#pragma unroll
  for (int i = 0; i < 3; ++i) { const unsigned t = fh ? i + 1 : 0; rdQ[i] = 22528 + (8 * h + q4) * 256 + ((t ^ q4) * 64) + blk16 * 32 + p4 * 8; }
  const unsigned lrow = lane >> 3, ch2 = (lane & 7) ^ (((lrow >> 1) & 1) << 2);
  const unsigned voff2 = lrow * (D_ * 2) + ch2 * 16;
  auto dma2j = [&](int q, unsigned j) {
    const int hh = (2 * q) % 6;
    const unsigned so = j >= 22 ? ((hh + 1) % 6) * SL + (j - 22) * 1024 : hh * SL + j * 1024;
    if (j < 36) glds16s(voff2, wd + (size_t)(288 * c + 8 * j) * (D_ * 2) + q * 128, l0 + so);
    else { const unsigned l = fresh_lane(), lr = l >> 3; unsigned wr = 36 * c + 8 * (j - 36) + lr; if (wr > 575) wr = 575;
      glds16s(wr * (D_ * 2) + (((l & 7) ^ (((lr >> 1) & 1) << 2)) * 16), wo + q * 128, l0 + so); }
  };
  auto dma2_A = [&](int q) {
#pragma unroll
    for (int k = 0; k < 5; ++k) dma2j(q, tg + 4 * k);
  };
  auto dma2_Bodd = [&](int q) {
#pragma unroll
    for (int k = 0; k < 5; ++k) dma2j(q, 22 + tg + 4 * k);
  };
  auto dma2_Beven = [&](int q) { if (tg < 2) dma2j(q, 20 + tg); };
  auto dma2_all = [&](int q) { if (fh) { dma2_Beven(q); dma2_Bodd(q); } else dma2_A(q); };
  auto l2_prefetch = [&]() {
    unsigned idx = tg * 64 + fresh_lane();
    const char* a;
    if (idx < 162) a = wd + (size_t)(288 * c + 18 * mt) * (D_ * 2) + idx * 128;
    else { unsigned k = idx - 162; if (k > 20) k = 20; k += mt * 21; if (k > 323) k = 323; a = wo + (size_t)(36 * c) * (D_ * 2) + k * 128; }
    asm volatile("s_mov_b32 m0, %1\n\ts_nop 0\n\tglobal_load_lds_dword %0, off" :: "v"(a), "s"(l0 + NSL * SL + wid * 256) : "memory", "m0");
  };
  const u64 epoch = fl_load(FL(0));
  if (fh) {
#pragma unroll 1
    for (int hh = 0; hh < 4; ++hh)
#pragma unroll
      for (int k = 0; k < 5; ++k) if (tg + 4 * k < 18) dma_piece(P, tg + 4 * k, hh, l0 + hh * SL);
  } else if (wid == 0) {
    const int j = lane & 7, t = 128 * mt + 8 * c + (lane >> 3);
    const u32x4* src = (const u32x4*)(x + (size_t)t * D_) + j;
    u32x4 v[9];
#pragma unroll
    for (int i = 0; i < 9; ++i) v[i] = src[8 * i];
    float s = 0, q = 0;
#pragma unroll
    for (int i = 0; i < 9; ++i)
#pragma unroll
      for (int e = 0; e < 4; ++e) { s = dot2(v[i][e], 0x3f803f80u, s); q = dot2(v[i][e], v[i][e], q); }
#pragma unroll
    for (int o = 1; o < 8; o <<= 1) { s += __shfl_xor(s, o); q += __shfl_xor(q, o); }
    const float m = s * (1.f / D_);
    const float var = fmaxf(q * (1.f / D_) - m * m, 0.f);
    const float mb = rbf(m), vb = rbf(var);
    const float ri = 1.f / rbf(sqrtf(rbf(vb + 1e-5f)));
    const f32x2 mb2 = {mb, mb}, ri2 = {ri, ri};
    char* gdst = xf + (size_t)((t >> 5) * NKS) * 1024 + (t & 31) * 16 + (j & 1) * 512 + (j >> 1) * 1024;
#pragma unroll
    for (int i = 0; i < 9; ++i) {
      u32x4 o;
#pragma unroll
      for (int e = 0; e < 4; ++e) { f32x2 a = {lo_f(v[i][e]), hi_f(v[i][e])}; a -= mb2; const unsigned d = cvtpk(a[0], a[1]); f32x2 b = {lo_f(d), hi_f(d)}; b *= ri2; o[e] = cvtpk(b[0], b[1]); }
      ST128(gdst + i * 4096, o);
    }
    asm volatile("s_waitcnt vmcnt(0)" ::: "memory");
  } else {
    u32x2 w[6];
#pragma unroll
    for (int i = 0; i < 6; ++i) {
      const unsigned id = ((wid - 1) * 6 + i) * 64 + lane, row = 36 * mt + (id >> 5), qd = id & 31, pt = qd / 9;
      w[i] = u32x2{0u, 0u};
      if (qd < 27) w[i] = *(const u32x2*)(wqkv + (size_t)row * 3456 + pt * 1152 + 72 * c + (qd - 9 * pt) * 8);
    }
#pragma unroll
    for (int i = 0; i < 6; ++i) {
      const unsigned id = ((wid - 1) * 6 + i) * 64 + lane, row = 36 * mt + (id >> 5), qd = id & 31;
      ST64(wqp + (size_t)(c * 576 + row) * 256 + (((qd >> 3) ^ (row & 3)) * 64) + (qd & 7) * 8, w[i]);
    }
    asm volatile("s_waitcnt vmcnt(0)" ::: "memory");
  }
  const u64 tag = (((u64)(unsigned)__builtin_amdgcn_readfirstlane((unsigned)(epoch >> 32)) << 32) | (u64)(unsigned)__builtin_amdgcn_readfirstlane((unsigned)epoch)) + 1ull;
  STAMP(11);
  __builtin_amdgcn_s_barrier();
  STAMP(4);
  if (wid == 0) {
    if (lane == 0) fl_store(FL(FL_LN + mt * 16 + c), tag);
    fl_wait(FL(FL_LN + ((lane & 16) ? (lane & 15) * 16 + c : mt * 16 + (lane & 15))), tag);
    STAMP(12);
  }
  __builtin_amdgcn_s_barrier();
  STAMP(13);
#pragma unroll
  for (int k = 0; k < 5; ++k) { const unsigned i = wid + 8 * k; if (i < 32) dma_piece(P, 18 + (i & 7), i >> 3, l0 + (i >> 3) * SL); else if (i < 38) dma_piece(P, 20 + (i - 32), 4, l0 + 4 * SL); }
  wait_bar<0>();
  STAMP(1);
  const f32x16 zero = {};
  f32x16 acc[11];
#pragma unroll
  for (int i = 0; i < 11; ++i) acc[i] = zero;
  if (fh) phase1<1>([&]() { dma2_all(0); }, l2_prefetch, P, tg, l0, ldsb, rdA, rdX, rdQ, acc); else phase1<0>([&]() { dma2_all(0); }, l2_prefetch, P, tg, l0, ldsb, rdA, rdX, rdQ, acc);
  u32x4 own[10];
#pragma unroll
  for (int b = 0; b < 5; ++b) if (b < 4 || fh == 0)
#pragma unroll
    for (int i = 0; i < 8; ++i) {
      const unsigned gp = cvtpk(fmaxf(acc[2 * b][2 * i], 0.f), fmaxf(acc[2 * b][2 * i + 1], 0.f)), up = cvtpk(acc[2 * b + 1][2 * i], acc[2 * b + 1][2 * i + 1]);
      own[2 * b + (i >> 2)][i & 3] = cvtpk(lo_f(gp) * lo_f(up), hi_f(gp) * hi_f(up));
    }
  if (wid == 7) {
    char* hb = halo + (size_t)(mt * 16 + c) * 4096 + ((lane & 15) * 144);
    if (lane & 16) {
#pragma unroll
      for (int t = 1; t < 4; ++t)
#pragma unroll
        for (int g = 0; g < 4; ++g) {
          const int c0 = 32 * t + 8 * g;
          if (c0 + 4 >= 36 && c0 < 108) {
            const u32x2 w = {cvtpk(acc[7 + t][4 * g], acc[7 + t][4 * g + 1]), cvtpk(acc[7 + t][4 * g + 2], acc[7 + t][4 * g + 3])};
            const int cc = c0 + 4 * (int)h;
            if (cc >= 36 && cc < 108) asm volatile("global_store_dwordx2 %0, %1, off sc0 sc1" :: "v"(hb + (cc - 36) * 2), "v"(w) : "memory");
          }
        }
    }
  }
  bar_l();
  STAMP(2);
  if (wid == 7) {
    asm volatile("s_waitcnt vmcnt(0)" ::: "memory");
    if (lane == 0) fl_store(FL(FL_HALO + mt * 16 + c), tag);
  }
  constexpr int HX = 2 * SL, QK = HX + 73728;
  const unsigned hxo = HX + tg * 18432 + lane * 16;
  {
    const lds_ptr hx = (lds_ptr)lds + hxo + fh * 10240;
#pragma unroll
    for (int i = 0; i < 10; ++i) if (i < 8 || fh == 0) *(LAS u32x4*)(hx + i * 1024) = own[i];
    lds_ptr d = (lds_ptr)lds + QK + (16 + tg * 32 + (lane & 31)) * QP + 8 * h;
    auto put = [&](const f32x16& a, int t) {
#pragma unroll
      for (int g = 0; g < 4; ++g) if (32 * t + 8 * g < 108) {
        const u32x2 w = {cvtpk(a[4 * g], a[4 * g + 1]), cvtpk(a[4 * g + 2], a[4 * g + 3])};
        if (32 * t + 8 * g + 4 < 108 || h == 0) *(LAS u32x2*)(d + (32 * t + 8 * g) * 2) = w;
      }
    };
    if (fh) { put(acc[8], 1); put(acc[9], 2); put(acc[10], 3); } else put(acc[10], 0);
  }
  if (mt > 0 && wid == 0) {
    fl_wait(FL(FL_HALO + (mt - 1) * 16 + c), tag);
    const char* hb = halo + (size_t)((mt - 1) * 16 + c) * 4096;
#pragma unroll
    for (int i = 0; i < 3; ++i) {
      const unsigned id = lane + 64 * i;
      if (id < 144) {
        u32x4 v;
        asm volatile("global_load_dwordx4 %0, %1, off sc0 sc1\n\ts_waitcnt vmcnt(0)" : "=&v"(v) : "v"(hb + id * 16) : "memory");
        const unsigned tk = id / 9, ck = id - tk * 9;
        lds_ptr d = (lds_ptr)lds + QK + tk * QP + 72 + ck * 16;
        *(LAS u32x2*)d = u32x2{v[0], v[1]}; *(LAS u32x2*)(d + 8) = u32x2{v[2], v[3]};
      }
    }
  }
  if (mt == 0 && wid == 0) {
#pragma unroll
    for (int i = 0; i < 8; ++i) { const unsigned id = lane + 64 * i; if (id < 16 * (QP / 8)) *(LAS u32x2*)((lds_ptr)lds + QK + id * 8) = u32x2{0u, 0u}; }
  }
  bar_l();
  STAMP(3);
  bf16x8 hf[21];
#pragma unroll
  for (int s = 0; s < 18; ++s) hf[s] = *(const LAS bf16x8*)(ldsb + hxo + s * 1024);
  STAMP(5);
  float r10[10];
  const int tok = (tid & 31) + 32 * ((tid >> 6) & 3), wp = (tid >> 5) & 1; const int head = tid >> 8;
  {
    const int t = 128 * mt + tok, slot = 16 + tok;
    const int ox = head * 40, oy = head ? 36 : 32;
    const lds_cptr qp = ldsb + QK + slot * QP;
    unsigned qv[9];
#pragma unroll
    for (int i = 0; i < 4; ++i) { const u32x2 u = *(const LAS u32x2*)(qp + ox + 8 * i); qv[2 * i] = u[0]; qv[2 * i + 1] = u[1]; }
    qv[8] = *(const LAS unsigned*)(qp + oy);
    float lg[4];
#pragma unroll
    for (int i4 = 0; i4 < 4; ++i4) {
      const int w = 4 * wp + i4;
      const lds_cptr kp = qp - 2 * w * QP + 72;
      float a = 0;
#pragma unroll
      for (int i = 0; i < 4; ++i) { const u32x2 u = *(const LAS u32x2*)(kp + ox + 8 * i); a = dot2(qv[2 * i], u[0], a); a = dot2(qv[2 * i + 1], u[1], a); }
      a = dot2(qv[8], *(const LAS unsigned*)(kp + oy), a);
      lg[i4] = (t - 2 * w >= 0) ? rbf(rbf(a) * 0.23570226039551584f) : -1e9f;
    }
    float mx = fmaxf(fmaxf(lg[0], lg[1]), fmaxf(lg[2], lg[3]));
    mx = fmaxf(mx, __shfl_xor(mx, 32));
    float sum = 0;
#pragma unroll
    for (int i = 0; i < 4; ++i) { lg[i] = __expf(lg[i] - mx); sum += lg[i]; }
    sum += __shfl_xor(sum, 32);
    const float inv = 1.f / sum;
    f32x2 o[9];
#pragma unroll
    for (int i = 0; i < 9; ++i) o[i] = f32x2{0.f, 0.f};
#pragma unroll
    for (int i4 = 0; i4 < 4; ++i4) {
      const int w = 4 * wp + i4;
      const float p = rbf(lg[i4] * inv); const f32x2 p2 = {p, p};
      const lds_cptr vp = qp - 2 * w * QP + 144;
#pragma unroll
      for (int i = 0; i < 4; ++i) { const u32x2 u = *(const LAS u32x2*)(vp + ox + 8 * i);
        o[2 * i] += p2 * f32x2{lo_f(u[0]), hi_f(u[0])}; o[2 * i + 1] += p2 * f32x2{lo_f(u[1]), hi_f(u[1])}; }
      const unsigned y = *(const LAS unsigned*)(vp + oy);
      o[8] += p2 * f32x2{lo_f(y), hi_f(y)};
    }
#pragma unroll
    for (int i = 0; i < 5; ++i) {
      f32x2 a = (i < 4) ? o[i] : o[8], b = (i < 4) ? o[4 + i] : f32x2{0.f, 0.f};
      asm volatile("" : "+v"(a), "+v"(b));
      const f32x2 send = wp ? a : b, keep = wp ? b : a;
      r10[2 * i] = keep[0] + __shfl_xor(send[0], 32); r10[2 * i + 1] = keep[1] + __shfl_xor(send[1], 32);
    }
  }
  bar_l();
  {
    lds_ptr op = (lds_ptr)lds + HX + tok * 104 + head * 36;
#pragma unroll
    for (int i = 0; i < 4; ++i) *(LAS unsigned*)(op + 4 * (head + 4 * wp + i)) = cvtpk(r10[2 * i], r10[2 * i + 1]);
    if (wp == 0) *(LAS unsigned*)(op + (head ? 0 : 32)) = cvtpk(r10[8], r10[9]);
    if (head == 1 && wp == 1) { lds_ptr z = (lds_ptr)lds + HX + tok * 104 + 72; *(LAS u32x2*)z = u32x2{0u, 0u}; *(LAS u32x2*)(z + 8) = u32x2{0u, 0u}; *(LAS u32x2*)(z + 16) = u32x2{0u, 0u}; }
  }
  bar_l();
  STAMP(6);
#pragma unroll
  for (int s = 0; s < 3; ++s) {
    const lds_cptr p = ldsb + HX + (tg * 32 + (lane & 31)) * 104 + (16 * s + 4 * h) * 2;
    const u32x2 a = *(const LAS u32x2*)p, b = *(const LAS u32x2*)(p + 16);
    hf[18 + s] = __builtin_bit_cast(bf16x8, u32x4{a[0], a[1], b[0], b[1]});
  }
  wait_bar_l<0>();
  dma2_all(1); dma2_all(2);
  STAMP(7);
  const unsigned colb = (((4 * fh + 2 * blk16 + (p4 >> 1)) ^ ((q4 >> 1) << 2)) * 16) + 8 * (p4 & 1);
  const lds_cptr rb2 = ldsb + (4 * h + q4) * 128 + colb;
  unsigned short* pp = part + (((size_t)(mt * 4 + tg) * 18 + fh) * 32 + c) * 512 + lane * 8;
  {
    constexpr int LA = 6, RING = 7, CNT = 21;
    bf16x8 fr[RING];
    auto rd2 = [&](int q, int n) -> bf16x8 {
      const int hh = (2 * q + (n >= 11 ? 1 : 0)) % 6, nn = n >= 11 ? n - 11 : n;
      return trfrag(rb2 + hh * SL, nn * 2048, nn * 2048 + 1024);
    };
#pragma unroll
    for (int n = 0; n < LA; ++n) { fr[n] = rd2(0, n); SBAR(); }
    f32x16 acc2[2];
#pragma unroll
    for (int q = 0; q < 9; ++q) {
#pragma unroll
      for (int n = 0; n < CNT; ++n) {
        if (n == CNT - LA && q < 8) {
          if (!fh && q >= 1 && q + 2 < 9) dma2_A(q + 2);
          if (fh) {
            if (q == 0) { if (tg < 2) wait_bar<6>(); else wait_bar<5>(); }
            else if (q < 7) { if (tg < 2) wait_bar<3>(); else wait_bar<2>(); }
            else wait_bar<2>();
          } else {
            if (q == 0) wait_bar<5>(); else if (q < 7) wait_bar<7>(); else wait_bar<2>();
          }
          if (fh) { if (q >= 1 && q + 2 < 9) dma2_Bodd(q + 2); if (q + 3 < 9) dma2_Beven(q + 3); }
          if (q == 7 && tid == 64) fl_store(FL(FL_A + mt * 16 + c), tag);
        }
        if (n + LA < CNT) fr[(n + LA) % RING] = rd2(q, n + LA);
        else if (q < 8) fr[(n + LA) % RING] = rd2(q + 1, n + LA - CNT);
        acc2[q & 1] = MFMA(fr[n % RING], hf[n], n == 0 ? zero : acc2[q & 1]);
        if (n == 3 && q > 0) {
          u32x4 o0, o1;
#pragma unroll
          for (int i = 0; i < 4; ++i) { o0[i] = cvtpk(acc2[(q - 1) & 1][2 * i], acc2[(q - 1) & 1][2 * i + 1]); o1[i] = cvtpk(acc2[(q - 1) & 1][8 + 2 * i], acc2[(q - 1) & 1][8 + 2 * i + 1]); }
          PSTORE(pp + (q - 1) * 32768, o0); PSTORE(pp + (q - 1) * 32768 + 8192, o1);
        }
        SBAR();
      }
    }
    u32x4 o0, o1;
#pragma unroll
    for (int i = 0; i < 4; ++i) { o0[i] = cvtpk(acc2[0][2 * i], acc2[0][2 * i + 1]); o1[i] = cvtpk(acc2[0][8 + 2 * i], acc2[0][8 + 2 * i + 1]); }
    PSTORE(pp + 8 * 32768, o0); PSTORE(pp + 8 * 32768 + 8192, o1);
  }
  STAMP(8);
  asm volatile("s_waitcnt vmcnt(0)" ::: "memory");
  __builtin_amdgcn_s_barrier();
  if (tid == 0) fl_store(FL(FL_B + mt * 16 + c), tag);
  {
    fl_wait(FL((wid < 6 ? FL_A : FL_B) + mt * 16 + (lane & 15)), tag);
  }
  STAMP(9);
  {
    const unsigned wt = c >> 1, tb = mt * 4 + (wt & 3), hf2 = c & 1;
    auto reduce1 = [&](unsigned r, unsigned ln, u32x4 (&v)[NCH], u32x2& xa, u32x2& xb) {
      const unsigned db = 2 * r + (wt >> 2);
      const unsigned short* src = part + ((size_t)(tb * 18 + db) * 2 + hf2) * 8192 + ln * 8;
#pragma unroll
      for (int cc = 0; cc < NCH; ++cc) asm volatile("global_load_dwordx4 %0, %1, off sc0 sc1" : "=v"(v[cc]) : "v"(src + cc * 512) : "memory");
      const size_t off = (size_t)(tb * 32 + (ln & 31)) * D_ + db * 32 + hf2 * 16 + 4 * (ln >> 5);
      xa = *(const u32x2*)(x + off); xb = *(const u32x2*)(x + off + 8);
    };
#define WAITV(v) asm volatile("s_waitcnt vmcnt(0)" : "+v"(v[0]), "+v"(v[1]), "+v"(v[2]), "+v"(v[3]), "+v"(v[4]), "+v"(v[5]), "+v"(v[6]), "+v"(v[7]), "+v"(v[8]), "+v"(v[9]), "+v"(v[10]), "+v"(v[11]), "+v"(v[12]), "+v"(v[13]), "+v"(v[14]), "+v"(v[15]) :: "memory")
    auto finish1 = [&](unsigned r, unsigned ln, const u32x4 (&v)[NCH], const u32x2& xa, const u32x2& xb) {
      const unsigned db = 2 * r + (wt >> 2);
      const size_t off = (size_t)(tb * 32 + (ln & 31)) * D_ + db * 32 + hf2 * 16 + 4 * (ln >> 5);
      f32x2 a[4] = {{lo_f(xa[0]), hi_f(xa[0])}, {lo_f(xa[1]), hi_f(xa[1])}, {lo_f(xb[0]), hi_f(xb[0])}, {lo_f(xb[1]), hi_f(xb[1])}};
      f32x2 sm[4] = {{0.f, 0.f}, {0.f, 0.f}, {0.f, 0.f}, {0.f, 0.f}};
#pragma unroll
      for (int cc = 0; cc < NCH; ++cc) {
#pragma unroll
        for (int i = 0; i < 4; ++i) sm[i] += f32x2{lo_f(v[cc][i]), hi_f(v[cc][i])};
        if ((cc & 3) == 3) SBAR(); }
      u32x2 o0 = {cvtpk(a[0][0] + sm[0][0], a[0][1] + sm[0][1]), cvtpk(a[1][0] + sm[1][0], a[1][1] + sm[1][1])};
      u32x2 o1 = {cvtpk(a[2][0] + sm[2][0], a[2][1] + sm[2][1]), cvtpk(a[3][0] + sm[3][0], a[3][1] + sm[3][1])};
      *(u32x2*)(out + off) = o0; *(u32x2*)(out + off + 8) = o1;
    };
    u32x4 v[NCH]; u32x2 xa, xb;
    reduce1(wid, lane, v, xa, xb);
    if (wid >= 6) {
      const unsigned u = (wid - 6) * 64 + lane, ln = u >> 1, g = u & 1, db = 16 + (wt >> 2);
      const unsigned short* src = part + ((size_t)(tb * 18 + db) * 2 + hf2) * 8192 + ln * 8 + g * 4;
      u32x2 v2[NCH];
#pragma unroll
      for (int cc = 0; cc < NCH; ++cc) asm volatile("global_load_dwordx2 %0, %1, off sc0 sc1" : "=v"(v2[cc]) : "v"(src + cc * 512) : "memory");
      const size_t off = (size_t)(tb * 32 + (ln & 31)) * D_ + db * 32 + hf2 * 16 + 4 * (ln >> 5) + 8 * g;
      const u32x2 xa2 = *(const u32x2*)(x + off);
      WAITV(v); WAITV(v2);
      SBAR();
      finish1(wid, lane, v, xa, xb);
      SBAR();
      f32x2 s0 = {lo_f(xa2[0]), hi_f(xa2[0])}, s1 = {lo_f(xa2[1]), hi_f(xa2[1])}, t0 = {0.f, 0.f}, t1 = {0.f, 0.f};
#pragma unroll
      for (int cc = 0; cc < NCH; ++cc) { t0 += f32x2{lo_f(v2[cc][0]), hi_f(v2[cc][0])}; t1 += f32x2{lo_f(v2[cc][1]), hi_f(v2[cc][1])}; }
      *(u32x2*)(out + off) = u32x2{cvtpk(s0[0] + t0[0], s0[1] + t0[1]), cvtpk(s1[0] + t1[0], s1[1] + t1[1])};
    } else { WAITV(v); finish1(wid, lane, v, xa, xb); }
  }
  STAMP(10);
  if (blockIdx.x == 0 && wid == 0) {
#pragma unroll 1
    for (int i = 0; i < 4; ++i) fl_wait(FL(FL_B + i * 64 + lane), tag);
    if (lane == 0) fl_store(FL(0), tag);
  }
}

extern "C" void kernel_launch(void* const* d_in, const int* in_sizes, int n_in, void* d_out, int out_size, void* d_ws, size_t ws_size, hipStream_t stream) {
  const unsigned short* x = (const unsigned short*)d_in[0];
  const char* wqkv = (const char*)d_in[1];
  const char* wo = (const char*)d_in[2];
  const char* wg = (const char*)d_in[4]; const char* wu = (const char*)d_in[5]; const char* wd = (const char*)d_in[6];
  char* ws = (char*)d_ws;
  u64* fl64 = (u64*)ws;
  char* halo = ws + (1u << 20);
  char* xf = ws + (4u << 20);
  unsigned short* part = (unsigned short*)(ws + (8u << 20));
  char* wqp = ws + (48u << 20);
  (void)hipFuncSetAttribute((const void*)k_main, hipFuncAttributeMaxDynamicSharedMemorySize, LDS_BYTES);
  k_main<<<256, 512, LDS_BYTES, stream>>>(x, wqkv, wg, wu, wd, wo, part, halo, fl64, xf, wqp, (unsigned short*)d_out);
}
```

```cpp
#include <hip/hip_runtime.h>
#include <hip/hip_bf16.h>
#include <stdint.h>
#include <utility>
#include <type_traits>

using bf16x8 = __attribute__((ext_vector_type(8))) short;
using s16x4  = __attribute__((ext_vector_type(4))) short;
using f32x16 = __attribute__((ext_vector_type(16))) float;
using f32x2  = __attribute__((ext_vector_type(2))) float;
using u32x4  = __attribute__((ext_vector_type(4))) unsigned;
using u32x2  = __attribute__((ext_vector_type(2))) unsigned;
#define LAS __attribute__((address_space(3)))
typedef LAS const char* lds_cptr;
typedef LAS char* lds_ptr;
typedef short v4i16_t __attribute__((ext_vector_type(4)));
typedef __bf16 bf16x2_t __attribute__((ext_vector_type(2)));
#define DI __device__ __forceinline__
#define SBAR() __builtin_amdgcn_sched_barrier(0)

constexpr int S_ = 2048, D_ = 576, F_ = 4608;
constexpr int NKS = 36;
constexpr int NCH = 16;
constexpr int SL = 26624;
constexpr int NSL = 6;
constexpr int LDS_BYTES = 163840;
constexpr int QP = 232;

DI unsigned cvtpk(float lo, float hi) { f32x2 v = {lo, hi}; bf16x2_t b = __builtin_convertvector(v, bf16x2_t); return __builtin_bit_cast(unsigned, b); }
DI float rbf(float f) { return __uint_as_float(cvtpk(f, 0.f) << 16); }
DI float lo_f(unsigned p) { return __uint_as_float(p << 16); }
DI float hi_f(unsigned p) { return __uint_as_float(p & 0xffff0000u); }
DI float dot2(unsigned a, unsigned b, float c) { return __builtin_amdgcn_fdot2_f32_bf16(__builtin_bit_cast(bf16x2_t, a), __builtin_bit_cast(bf16x2_t, b), c, false); }
DI s16x4 vtr(lds_cptr p) { return __builtin_bit_cast(s16x4, __builtin_amdgcn_ds_read_tr16_b64_v4i16((LAS v4i16_t*)p)); }
DI bf16x8 trfrag(lds_cptr p, int o0, int o1) { s16x4 a = vtr(p + o0); SBAR(); s16x4 b = vtr(p + o1); return __builtin_shufflevector(a, b, 0, 1, 2, 3, 4, 5, 6, 7); }
DI void glds16s(unsigned voff, const void* sbase, unsigned lds_base) {
  asm volatile("s_mov_b32 m0, %2\n\ts_nop 0\n\tglobal_load_lds_dwordx4 %0, %1" :: "v"(voff), "s"(sbase), "s"(lds_base) : "memory", "m0");
}
#define PSTORE(p, v) asm volatile("global_store_dwordx4 %0, %1, off sc0 sc1\n\ts_nop 1" :: "v"(p), "v"(v) : "memory")
#define MFMA(a, b, c) __builtin_amdgcn_mfma_f32_32x32x16_bf16((a), (b), (c), 0, 0, 0)
template <int N> DI void wait_bar() { asm volatile("s_waitcnt vmcnt(%0)\n\ts_barrier" :: "i"(N) : "memory"); }
template <int N> DI void wait_bar_l() { asm volatile("s_waitcnt vmcnt(%0) lgkmcnt(0)\n\ts_barrier" :: "i"(N) : "memory"); }
DI void bar_l() { asm volatile("s_waitcnt lgkmcnt(0)\n\ts_barrier" ::: "memory"); }
template <int C> DI lds_cptr vaddc(unsigned v) { unsigned r; if (C == 0) r = v; else asm volatile("v_add_u32_e32 %0, %2, %1" : "=v"(r) : "v"(v), "i"(C)); return (lds_cptr)(size_t)r; }
DI float relu1(float x) { float r; asm("v_max_f32_e32 %0, 0, %1" : "=v"(r) : "v"(x)); return r; }
DI unsigned fresh_lane() { unsigned l; asm volatile("v_mbcnt_lo_u32_b32 %0, -1, 0\n\tv_mbcnt_hi_u32_b32 %0, -1, %0" : "=v"(l)); return l; }

#define STAMP(i)

typedef unsigned long long u64;
#define FL(i) (fl64 + (size_t)(i) * 16)
constexpr int FL_LN = 1, FL_HALO = 257, FL_A = 513, FL_B = 769;
DI u64 fl_load(const u64* p) { return __hip_atomic_load(p, __ATOMIC_RELAXED, __HIP_MEMORY_SCOPE_AGENT); }
DI void fl_store(u64* p, u64 v) { __hip_atomic_store(p, v, __ATOMIC_RELAXED, __HIP_MEMORY_SCOPE_AGENT); }
DI void fl_wait(const u64* p, u64 tag) { unsigned spins = 0; while (!__all(fl_load(p) == tag) && ++spins < (1u << 22)) __builtin_amdgcn_s_sleep(1); asm volatile("" ::: "memory"); }
#define ST128(p, v) asm volatile("global_store_dwordx4 %0, %1, off sc0 sc1\n\ts_nop 1" :: "v"(p), "v"(v) : "memory")
#define ST64(p, v) asm volatile("global_store_dwordx2 %0, %1, off sc0 sc1" :: "v"(p), "v"(v) : "memory")

template <class F, int... I> DI void for_each_int(F&& f, std::integer_sequence<int, I...>) { (f(std::integral_constant<int, I>{}), ...); }
#define PFST 4
#define NPF 1
struct Ptrs { const char* wg; const char* wu; const char* xm; const char* wq; };

DI void dma_piece(const Ptrs& P, unsigned p, unsigned hh, unsigned ldsdst) {
  const unsigned l = fresh_lane();
  if (p < 18) {
    const unsigned mat = p >= 9 ? 1u : 0u, pj = p - 9 * mat;
    const unsigned g = l + 64 * pj, row = (g * 1821u) >> 16;
    glds16s(row * 8640u + g * 16u, (mat ? P.wu : P.wg) + (size_t)hh * (16 * F_ * 2), ldsdst + p * 1024);
  } else if (p < 22) {
    glds16s(l * 16u, P.xm + (size_t)((p - 18) * NKS + hh) * 1024, ldsdst + p * 1024);
  } else {
    glds16s(l * 16u, P.wq + (size_t)hh * 4096 + (p - 22) * 1024, ldsdst + p * 1024);
  }
}

template <int FH, class F, class G> DI void phase1(F&& tail_dma, G&& l2_prefetch, const Ptrs& P, const unsigned w4, const unsigned l0, lds_cptr ldsb, const unsigned rdA, const unsigned rdX, const unsigned (&rdQ)[3], f32x16 (&acc)[11]) {
  constexpr int LA = 5, RING = 6;
  auto issue_odd = [&](unsigned hh, unsigned so) {
#pragma unroll
    for (int k = 0; k < 7; ++k) if (k < 6 || w4 < 2) dma_piece(P, w4 + 4 * k, hh, l0 + so);
  };
  auto issue_evenB = [&](unsigned hh, unsigned so) {
    if (w4 < 2) dma_piece(P, 20 + w4, hh, l0 + so); else { dma_piece(P, 18 + 2 * w4, hh, l0 + so); dma_piece(P, 19 + 2 * w4, hh, l0 + so); }
  };
  auto issue_evenA = [&](unsigned hh, unsigned so) {
#pragma unroll
    for (int k = 0; k < 5; ++k) dma_piece(P, w4 + 4 * k, hh, l0 + so);
  };
  bf16x8 fr[RING]; bf16x8 xq[2];
  constexpr int NGU = FH ? 8 : 10;
  lds_cptr aG = nullptr, aQ[3] = {nullptr, nullptr, nullptr};
  const unsigned rA = l0 + rdA, rX = l0 + rdX, rQ0 = l0 + rdQ[0], rQ1 = l0 + rdQ[1], rQ2 = l0 + rdQ[2];
#define SET_SLOT(slot) do { aG = vaddc<(slot) * SL>(rA); aQ[0] = vaddc<(slot) * SL>(rQ0); if (FH) { aQ[1] = vaddc<(slot) * SL>(rQ1); aQ[2] = vaddc<(slot) * SL>(rQ2); } } while (0)
#define RD(dst, n) do { constexpr int nn_ = (n) % 66, slot_ = nn_ / 11, t_ = nn_ % 11; if (t_ == 0) SET_SLOT(slot_); \
    if (t_ < NGU) dst = trfrag(aG, (t_ & 1) * 9216 + (t_ >> 1) * 64, (t_ & 1) * 9216 + (t_ >> 1) * 64 + 4 * 576); else dst = trfrag(aQ[t_ < NGU ? 0 : t_ - NGU], 0, 4 * 256); } while (0)
#define RDX(slot) (*(const LAS bf16x8*)vaddc<(slot) * SL>(rX))
  xq[0] = RDX(0);
  SBAR();
  RD(fr[0], 0); SBAR(); RD(fr[1], 1); SBAR(); RD(fr[2], 2); SBAR(); RD(fr[3], 3); SBAR(); RD(fr[4], 4); SBAR();
#pragma unroll 1
  for (int it = 0; it < 6; ++it) {
    auto step = [&](auto n_) {
      constexpr int n = decltype(n_)::value;
      constexpr int S3 = n / 22, m = n % 22;
      const int st = 3 * it + S3;
      if (m == 22 - LA) {
        if (!FH && st <= 15) issue_evenA(2 * st + 4, ((2 * S3 + 4) % 6) * SL);
        if (st < 17) {
          if (st == 16) wait_bar<0>();
          else if (FH) { if (st == PFST + 1) { if (w4 < 2) wait_bar<1 + NPF>(); else wait_bar<2 + NPF>(); } else { if (w4 < 2) wait_bar<1>(); else wait_bar<2>(); } }
          else wait_bar<5>();
        }
        xq[0] = RDX((2 * S3 + 2) % 6);
        if (FH) { if (st <= 15) issue_odd(2 * st + 5, ((2 * S3 + 5) % 6) * SL); if (st <= 14) issue_evenB(2 * st + 6, ((2 * S3 + 6) % 6) * SL); }
        if (S3 == 1 && st == 16) tail_dma();
        if (FH && st == PFST) l2_prefetch();
      }
      if (m == 11 - LA) xq[1] = RDX(2 * S3 + 1);
      const bf16x8 xcur = xq[m / 11];
      RD(fr[(n + LA) % RING], n + LA);
      acc[m % 11] = MFMA(fr[n % RING], xcur, acc[m % 11]);
      SBAR();
    };
    for_each_int(step, std::make_integer_sequence<int, 66>{});
  }
#undef RD
#undef RDX
#undef SET_SLOT
}

__global__ __launch_bounds__(512, 2) void k_main(const unsigned short* __restrict__ x, const char* __restrict__ wqkv, const char* __restrict__ wg, const char* __restrict__ wu,
                                                 const char* __restrict__ wd, const char* __restrict__ wo, unsigned short* __restrict__ part,
                                                 char* __restrict__ halo, u64* __restrict__ fl64, char* __restrict__ xf, char* __restrict__ wqp,
                                                 unsigned short* __restrict__ out) {
  extern __shared__ __attribute__((aligned(16))) char lds[];
  STAMP(0);
  const int tid = threadIdx.x, lane = tid & 63;
  const unsigned wid = __builtin_amdgcn_readfirstlane(tid >> 6);
  const unsigned tg = wid & 3, fh = wid >> 2;
  const unsigned bx = blockIdx.x, xcd = bx & 7, kk = bx >> 3;
  const unsigned c = xcd * 2 + (kk & 1), mt = kk >> 1;
  const unsigned l0 = (unsigned)(uintptr_t)lds;
  const lds_cptr ldsb = (lds_cptr)lds;
  Ptrs P;
  P.wg = wg + 576 * c; P.wu = wu + 576 * c; P.xm = xf + (size_t)(mt * 4) * NKS * 1024; P.wq = wqp + (size_t)c * (576 * 256);
  const unsigned h = lane >> 5, blk16 = (lane >> 4) & 1, q4 = (lane & 15) >> 2, p4 = lane & 3;
  const unsigned rdA = (8 * h + q4) * 576 + blk16 * 32 + p4 * 8 + fh * 5 * 64;
  const unsigned rdX = 18432 + tg * 1024 + lane * 16;
  unsigned rdQ[3];
#pragma unroll
  for (int i = 0; i < 3; ++i) { const unsigned t = fh ? i + 1 : 0; rdQ[i] = 22528 + (8 * h + q4) * 256 + ((t ^ q4) * 64) + blk16 * 32 + p4 * 8; }
  const unsigned lrow = lane >> 3, ch2 = (lane & 7) ^ (((lrow >> 1) & 1) << 2);
  const unsigned voff2 = lrow * (D_ * 2) + ch2 * 16;
  auto dma2j = [&](int q, unsigned j) {
    const int hh = (2 * q) % 6;
    const unsigned so = j >= 22 ? ((hh + 1) % 6) * SL + (j - 22) * 1024 : hh * SL + j * 1024;
    if (j < 36) glds16s(voff2, wd + (size_t)(288 * c + 8 * j) * (D_ * 2) + q * 128, l0 + so);
    else { const unsigned l = fresh_lane(), lr = l >> 3; unsigned wr = 36 * c + 8 * (j - 36) + lr; if (wr > 575) wr = 575;
      glds16s(wr * (D_ * 2) + (((l & 7) ^ (((lr >> 1) & 1) << 2)) * 16), wo + q * 128, l0 + so); }
  };
  auto dma2_A = [&](int q) {
#pragma unroll
    for (int k = 0; k < 5; ++k) dma2j(q, tg + 4 * k);
  };
  auto dma2_Bodd = [&](int q) {
#pragma unroll
    for (int k = 0; k < 5; ++k) dma2j(q, 22 + tg + 4 * k);
  };
  auto dma2_Beven = [&](int q) { if (tg < 2) dma2j(q, 20 + tg); };
  auto dma2_all = [&](int q) { if (fh) { dma2_Beven(q); dma2_Bodd(q); } else dma2_A(q); };
  auto l2_prefetch = [&]() {
    unsigned idx = tg * 64 + fresh_lane();
    const char* a;
    if (idx < 162) a = wd + (size_t)(288 * c + 18 * mt) * (D_ * 2) + idx * 128;
    else { unsigned k = idx - 162; if (k > 20) k = 20; k += mt * 21; if (k > 323) k = 323; a = wo + (size_t)(36 * c) * (D_ * 2) + k * 128; }
    asm volatile("s_mov_b32 m0, %1\n\ts_nop 0\n\tglobal_load_lds_dword %0, off" :: "v"(a), "s"(l0 + NSL * SL + wid * 256) : "memory", "m0");
  };
  const u64 epoch = fl_load(FL(0));
  if (fh) {
#pragma unroll 1
    for (int hh = 0; hh < 4; ++hh)
#pragma unroll
      for (int k = 0; k < 5; ++k) if (tg + 4 * k < 18) dma_piece(P, tg + 4 * k, hh, l0 + hh * SL);
  } else if (wid == 0) {
    const int j = lane & 7, t = 128 * mt + 8 * c + (lane >> 3);
    const u32x4* src = (const u32x4*)(x + (size_t)t * D_) + j;
    u32x4 v[9];
#pragma unroll
    for (int i = 0; i < 9; ++i) v[i] = src[8 * i];
    float s = 0, q = 0;
#pragma unroll
    for (int i = 0; i < 9; ++i)
#pragma unroll
      for (int e = 0; e < 4; ++e) { s = dot2(v[i][e], 0x3f803f80u, s); q = dot2(v[i][e], v[i][e], q); }
#pragma unroll
    for (int o = 1; o < 8; o <<= 1) { s += __shfl_xor(s, o); q += __shfl_xor(q, o); }
    const float m = s * (1.f / D_);
    const float var = fmaxf(q * (1.f / D_) - m * m, 0.f);
    const float mb = rbf(m), vb = rbf(var);
    const float ri = 1.f / rbf(sqrtf(rbf(vb + 1e-5f)));
    const f32x2 mb2 = {mb, mb}, ri2 = {ri, ri};
    char* gdst = xf + (size_t)((t >> 5) * NKS) * 1024 + (t & 31) * 16 + (j & 1) * 512 + (j >> 1) * 1024;
#pragma unroll
    for (int i = 0; i < 9; ++i) {
      u32x4 o;
#pragma unroll
      for (int e = 0; e < 4; ++e) { f32x2 a = {lo_f(v[i][e]), hi_f(v[i][e])}; a -= mb2; const unsigned d = cvtpk(a[0], a[1]); f32x2 b = {lo_f(d), hi_f(d)}; b *= ri2; o[e] = cvtpk(b[0], b[1]); }
      ST128(gdst + i * 4096, o);
    }
    asm volatile("s_waitcnt vmcnt(0)" ::: "memory");
  } else {
    u32x2 w[6];
#pragma unroll
    for (int i = 0; i < 6; ++i) {
      const unsigned id = ((wid - 1) * 6 + i) * 64 + lane, row = 36 * mt + (id >> 5), qd = id & 31, pt = qd / 9;
      w[i] = u32x2{0u, 0u};
      if (qd < 27) w[i] = *(const u32x2*)(wqkv + (size_t)row * 3456 + pt * 1152 + 72 * c + (qd - 9 * pt) * 8);
    }
#pragma unroll
    for (int i = 0; i < 6; ++i) {
      const unsigned id = ((wid - 1) * 6 + i) * 64 + lane, row = 36 * mt + (id >> 5), qd = id & 31;
      ST64(wqp + (size_t)(c * 576 + row) * 256 + (((qd >> 3) ^ (row & 3)) * 64) + (qd & 7) * 8, w[i]);
    }
    asm volatile("s_waitcnt vmcnt(0)" ::: "memory");
  }
  const u64 tag = (((u64)(unsigned)__builtin_amdgcn_readfirstlane((unsigned)(epoch >> 32)) << 32) | (u64)(unsigned)__builtin_amdgcn_readfirstlane((unsigned)epoch)) + 1ull;
  STAMP(11);
  __builtin_amdgcn_s_barrier();
  STAMP(4);
  if (wid == 0) {
    if (lane == 0) fl_store(FL(FL_LN + mt * 16 + c), tag);
    fl_wait(FL(FL_LN + ((lane & 16) ? (lane & 15) * 16 + c : mt * 16 + (lane & 15))), tag);
    STAMP(12);
  }
  __builtin_amdgcn_s_barrier();
  STAMP(13);
#pragma unroll
  for (int k = 0; k < 5; ++k) { const unsigned i = wid + 8 * k; if (i < 32) dma_piece(P, 18 + (i & 7), i >> 3, l0 + (i >> 3) * SL); else if (i < 38) dma_piece(P, 20 + (i - 32), 4, l0 + 4 * SL); }
  wait_bar<0>();
  STAMP(1);
  const f32x16 zero = {};
  f32x16 acc[11];
#pragma unroll
  for (int i = 0; i < 11; ++i) acc[i] = zero;
  if (fh) phase1<1>([&]() { dma2_all(0); }, l2_prefetch, P, tg, l0, ldsb, rdA, rdX, rdQ, acc); else phase1<0>([&]() { dma2_all(0); }, l2_prefetch, P, tg, l0, ldsb, rdA, rdX, rdQ, acc);
  const unsigned tag_lo = (unsigned)tag, tag_hi = (unsigned)(tag >> 32);
  if (wid == 7) {
    char* hb = halo + (size_t)(mt * 16 + c) * 8192 + ((lane & 15) * 288);
    if (lane & 16) {
#pragma unroll
      for (int t = 1; t < 4; ++t)
#pragma unroll
        for (int g = 0; g < 4; ++g) {
          const int c0 = 32 * t + 8 * g;
          if (c0 + 4 >= 36 && c0 < 108) {
            const u32x4 w = {cvtpk(acc[7 + t][4 * g], acc[7 + t][4 * g + 1]), cvtpk(acc[7 + t][4 * g + 2], acc[7 + t][4 * g + 3]), tag_lo, tag_hi};
            const int cc = c0 + 4 * (int)h;
            if (cc >= 36 && cc < 108) asm volatile("global_store_dwordx4 %0, %1, off sc0 sc1\n\ts_nop 1" :: "v"(hb + (cc - 36) * 4), "v"(w) : "memory");
          }
        }
    }
  }
  u32x4 own[10];
#pragma unroll
  for (int b = 0; b < 5; ++b) if (b < 4 || fh == 0)
#pragma unroll
    for (int i = 0; i < 8; ++i) {
      f32x2 g2 = {relu1(acc[2 * b][2 * i]), relu1(acc[2 * b][2 * i + 1])};
      g2 *= f32x2{acc[2 * b + 1][2 * i], acc[2 * b + 1][2 * i + 1]};
      own[2 * b + (i >> 2)][i & 3] = cvtpk(g2[0], g2[1]);
    }
  bar_l();
  STAMP(2);
  constexpr int HX = 2 * SL, QK = HX + 73728;
  const unsigned hxo = HX + tg * 18432 + lane * 16;
  {
    const lds_ptr hx = (lds_ptr)lds + hxo + fh * 10240;
#pragma unroll
    for (int i = 0; i < 10; ++i) if (i < 8 || fh == 0) *(LAS u32x4*)(hx + i * 1024) = own[i];
    lds_ptr d = (lds_ptr)lds + QK + (16 + tg * 32 + (lane & 31)) * QP + 8 * h;
    auto put = [&](const f32x16& a, int t) {
#pragma unroll
      for (int g = 0; g < 4; ++g) if (32 * t + 8 * g < 108) {
        const u32x2 w = {cvtpk(a[4 * g], a[4 * g + 1]), cvtpk(a[4 * g + 2], a[4 * g + 3])};
        if (32 * t + 8 * g + 4 < 108 || h == 0) *(LAS u32x2*)(d + (32 * t + 8 * g) * 2) = w;
      }
    };
    if (fh) { put(acc[8], 1); put(acc[9], 2); put(acc[10], 3); } else put(acc[10], 0);
  }
  if (mt > 0 && wid == 0) {
    const char* hb = halo + (size_t)((mt - 1) * 16 + c) * 8192;
    u32x4 v[5]; unsigned spins = 0;
    for (;;) {
#pragma unroll
      for (int i = 0; i < 5; ++i) { unsigned id = lane + 64 * i; if (id > 287) id = 287; asm volatile("global_load_dwordx4 %0, %1, off sc0 sc1" : "=v"(v[i]) : "v"(hb + id * 16) : "memory"); }
      asm volatile("s_waitcnt vmcnt(0)" : "+v"(v[0]), "+v"(v[1]), "+v"(v[2]), "+v"(v[3]), "+v"(v[4]) :: "memory");
      bool ok = true;
#pragma unroll
      for (int i = 0; i < 5; ++i) ok = ok && v[i][2] == tag_lo && v[i][3] == tag_hi;
      if (__all(ok) || ++spins >= (1u << 20)) break;
      __builtin_amdgcn_s_sleep(1);
    }
#pragma unroll
    for (int i = 0; i < 5; ++i) {
      const unsigned id = lane + 64 * i;
      if (id < 288) { const unsigned tk = id / 18, ck = id - tk * 18; *(LAS u32x2*)((lds_ptr)lds + QK + tk * QP + 72 + ck * 8) = u32x2{v[i][0], v[i][1]}; }
    }
  }
  if (mt == 0 && wid == 0) {
#pragma unroll
    for (int i = 0; i < 8; ++i) { const unsigned id = lane + 64 * i; if (id < 16 * (QP / 8)) *(LAS u32x2*)((lds_ptr)lds + QK + id * 8) = u32x2{0u, 0u}; }
  }
  bar_l();
  STAMP(3);
  bf16x8 hf[21];
#pragma unroll
  for (int s = 0; s < 18; ++s) hf[s] = *(const LAS bf16x8*)(ldsb + hxo + s * 1024);
  STAMP(5);
  float r10[10];
  const int tok = (tid & 31) + 32 * ((tid >> 6) & 3), wp = (tid >> 5) & 1; const int head = tid >> 8;
  {
    const int t = 128 * mt + tok, slot = 16 + tok;
    const int ox = head * 40, oy = head ? 36 : 32;
    const lds_cptr qp = ldsb + QK + slot * QP;
    unsigned qv[9];
#pragma unroll
    for (int i = 0; i < 4; ++i) { const u32x2 u = *(const LAS u32x2*)(qp + ox + 8 * i); qv[2 * i] = u[0]; qv[2 * i + 1] = u[1]; }
    qv[8] = *(const LAS unsigned*)(qp + oy);
    float lg[4];
#pragma unroll
    for (int i4 = 0; i4 < 4; ++i4) {
      const int w = 4 * wp + i4;
      const lds_cptr kp = qp - 2 * w * QP + 72;
      float a = 0;
#pragma unroll
      for (int i = 0; i < 4; ++i) { const u32x2 u = *(const LAS u32x2*)(kp + ox + 8 * i); a = dot2(qv[2 * i], u[0], a); a = dot2(qv[2 * i + 1], u[1], a); }
      a = dot2(qv[8], *(const LAS unsigned*)(kp + oy), a);
      lg[i4] = (t - 2 * w >= 0) ? rbf(rbf(a) * 0.23570226039551584f) : -1e9f;
    }
    float mx = fmaxf(fmaxf(lg[0], lg[1]), fmaxf(lg[2], lg[3]));
    mx = fmaxf(mx, __shfl_xor(mx, 32));
    float sum = 0;
#pragma unroll
    for (int i = 0; i < 4; ++i) { lg[i] = __expf(lg[i] - mx); sum += lg[i]; }
    sum += __shfl_xor(sum, 32);
    const float inv = 1.f / sum;
    f32x2 o[9];
#pragma unroll
    for (int i = 0; i < 9; ++i) o[i] = f32x2{0.f, 0.f};
#pragma unroll
    for (int i4 = 0; i4 < 4; ++i4) {
      const int w = 4 * wp + i4;
      const float p = rbf(lg[i4] * inv); const f32x2 p2 = {p, p};
      const lds_cptr vp = qp - 2 * w * QP + 144;
#pragma unroll
      for (int i = 0; i < 4; ++i) { const u32x2 u = *(const LAS u32x2*)(vp + ox + 8 * i);
        o[2 * i] += p2 * f32x2{lo_f(u[0]), hi_f(u[0])}; o[2 * i + 1] += p2 * f32x2{lo_f(u[1]), hi_f(u[1])}; }
      const unsigned y = *(const LAS unsigned*)(vp + oy);
      o[8] += p2 * f32x2{lo_f(y), hi_f(y)};
    }
#pragma unroll
    for (int i = 0; i < 5; ++i) {
      f32x2 a = (i < 4) ? o[i] : o[8], b = (i < 4) ? o[4 + i] : f32x2{0.f, 0.f};
      asm volatile("" : "+v"(a), "+v"(b));
      const f32x2 send = wp ? a : b, keep = wp ? b : a;
      r10[2 * i] = keep[0] + __shfl_xor(send[0], 32); r10[2 * i + 1] = keep[1] + __shfl_xor(send[1], 32);
    }
  }
  bar_l();
  {
    lds_ptr op = (lds_ptr)lds + HX + tok * 104 + head * 36;
#pragma unroll
    for (int i = 0; i < 4; ++i) *(LAS unsigned*)(op + 4 * (head + 4 * wp + i)) = cvtpk(r10[2 * i], r10[2 * i + 1]);
    if (wp == 0) *(LAS unsigned*)(op + (head ? 0 : 32)) = cvtpk(r10[8], r10[9]);
    if (head == 1 && wp == 1) { lds_ptr z = (lds_ptr)lds + HX + tok * 104 + 72; *(LAS u32x2*)z = u32x2{0u, 0u}; *(LAS u32x2*)(z + 8) = u32x2{0u, 0u}; *(LAS u32x2*)(z + 16) = u32x2{0u, 0u}; }
  }
  bar_l();
  STAMP(6);
#pragma unroll
  for (int s = 0; s < 3; ++s) {
    const lds_cptr p = ldsb + HX + (tg * 32 + (lane & 31)) * 104 + (16 * s + 4 * h) * 2;
    const u32x2 a = *(const LAS u32x2*)p, b = *(const LAS u32x2*)(p + 16);
    hf[18 + s] = __builtin_bit_cast(bf16x8, u32x4{a[0], a[1], b[0], b[1]});
  }
  wait_bar_l<0>();
  dma2_all(1); dma2_all(2);
  STAMP(7);
  const unsigned colb = (((4 * fh + 2 * blk16 + (p4 >> 1)) ^ ((q4 >> 1) << 2)) * 16) + 8 * (p4 & 1);
  const lds_cptr rb2 = ldsb + (4 * h + q4) * 128 + colb;
  unsigned short* pp = part + (((size_t)(mt * 4 + tg) * 18 + fh) * 32 + c) * 512 + lane * 8;
  {
    constexpr int LA = 6, RING = 7, CNT = 21;
    bf16x8 fr[RING];
    auto rd2 = [&](int q, int n) -> bf16x8 {
      const int hh = (2 * q + (n >= 11 ? 1 : 0)) % 6, nn = n >= 11 ? n - 11 : n;
      return trfrag(rb2 + hh * SL, nn * 2048, nn * 2048 + 1024);
    };
#pragma unroll
    for (int n = 0; n < LA; ++n) { fr[n] = rd2(0, n); SBAR(); }
    f32x16 acc2[2];
#pragma unroll
    for (int q = 0; q < 9; ++q) {
#pragma unroll
      for (int n = 0; n < CNT; ++n) {
        if (n == CNT - LA && q < 8) {
          if (!fh && q >= 1 && q + 2 < 9) dma2_A(q + 2);
          if (fh) {
            if (q == 0) { if (tg < 2) wait_bar<6>(); else wait_bar<5>(); }
            else if (q < 7) { if (tg < 2) wait_bar<3>(); else wait_bar<2>(); }
            else wait_bar<2>();
          } else {
            if (q == 0) wait_bar<5>(); else if (q < 7) wait_bar<7>(); else wait_bar<2>();
          }
          if (fh) { if (q >= 1 && q + 2 < 9) dma2_Bodd(q + 2); if (q + 3 < 9) dma2_Beven(q + 3); }
          if (q == 7 && tid == 64) fl_store(FL(FL_A + mt * 16 + c), tag);
        }
        if (n + LA < CNT) fr[(n + LA) % RING] = rd2(q, n + LA);
        else if (q < 8) fr[(n + LA) % RING] = rd2(q + 1, n + LA - CNT);
        acc2[q & 1] = MFMA(fr[n % RING], hf[n], n == 0 ? zero : acc2[q & 1]);
        if (n == 3 && q > 0) {
          u32x4 o0, o1;
#pragma unroll
          for (int i = 0; i < 4; ++i) { o0[i] = cvtpk(acc2[(q - 1) & 1][2 * i], acc2[(q - 1) & 1][2 * i + 1]); o1[i] = cvtpk(acc2[(q - 1) & 1][8 + 2 * i], acc2[(q - 1) & 1][8 + 2 * i + 1]); }
          PSTORE(pp + (q - 1) * 32768, o0); PSTORE(pp + (q - 1) * 32768 + 8192, o1);
        }
        SBAR();
      }
    }
    u32x4 o0, o1;
#pragma unroll
    for (int i = 0; i < 4; ++i) { o0[i] = cvtpk(acc2[0][2 * i], acc2[0][2 * i + 1]); o1[i] = cvtpk(acc2[0][8 + 2 * i], acc2[0][8 + 2 * i + 1]); }
    PSTORE(pp + 8 * 32768, o0); PSTORE(pp + 8 * 32768 + 8192, o1);
  }
  STAMP(8);
  asm volatile("s_waitcnt vmcnt(0)" ::: "memory");
  __builtin_amdgcn_s_barrier();
  if (tid == 0) fl_store(FL(FL_B + mt * 16 + c), tag);
  {
    fl_wait(FL((wid < 6 ? FL_A : FL_B) + mt * 16 + (lane & 15)), tag);
  }
  STAMP(9);
  {
    const unsigned wt = c >> 1, tb = mt * 4 + (wt & 3), hf2 = c & 1;
    auto reduce1 = [&](unsigned r, unsigned ln, u32x4 (&v)[NCH], u32x2& xa, u32x2& xb) {
      const unsigned db = 2 * r + (wt >> 2);
      const unsigned short* src = part + ((size_t)(tb * 18 + db) * 2 + hf2) * 8192 + ln * 8;
#pragma unroll
      for (int cc = 0; cc < NCH; ++cc) asm volatile("global_load_dwordx4 %0, %1, off sc0 sc1" : "=v"(v[cc]) : "v"(src + cc * 512) : "memory");
      const size_t off = (size_t)(tb * 32 + (ln & 31)) * D_ + db * 32 + hf2 * 16 + 4 * (ln >> 5);
      xa = *(const u32x2*)(x + off); xb = *(const u32x2*)(x + off + 8);
    };
#define WAITV(v) asm volatile("s_waitcnt vmcnt(0)" : "+v"(v[0]), "+v"(v[1]), "+v"(v[2]), "+v"(v[3]), "+v"(v[4]), "+v"(v[5]), "+v"(v[6]), "+v"(v[7]), "+v"(v[8]), "+v"(v[9]), "+v"(v[10]), "+v"(v[11]), "+v"(v[12]), "+v"(v[13]), "+v"(v[14]), "+v"(v[15]) :: "memory")
    auto finish1 = [&](unsigned r, unsigned ln, const u32x4 (&v)[NCH], const u32x2& xa, const u32x2& xb) {
      const unsigned db = 2 * r + (wt >> 2);
      const size_t off = (size_t)(tb * 32 + (ln & 31)) * D_ + db * 32 + hf2 * 16 + 4 * (ln >> 5);
      f32x2 a[4] = {{lo_f(xa[0]), hi_f(xa[0])}, {lo_f(xa[1]), hi_f(xa[1])}, {lo_f(xb[0]), hi_f(xb[0])}, {lo_f(xb[1]), hi_f(xb[1])}};
      f32x2 sm[4] = {{0.f, 0.f}, {0.f, 0.f}, {0.f, 0.f}, {0.f, 0.f}};
#pragma unroll
      for (int cc = 0; cc < NCH; ++cc) {
#pragma unroll
        for (int i = 0; i < 4; ++i) sm[i] += f32x2{lo_f(v[cc][i]), hi_f(v[cc][i])};
        if ((cc & 3) == 3) SBAR(); }
      u32x2 o0 = {cvtpk(a[0][0] + sm[0][0], a[0][1] + sm[0][1]), cvtpk(a[1][0] + sm[1][0], a[1][1] + sm[1][1])};
      u32x2 o1 = {cvtpk(a[2][0] + sm[2][0], a[2][1] + sm[2][1]), cvtpk(a[3][0] + sm[3][0], a[3][1] + sm[3][1])};
      *(u32x2*)(out + off) = o0; *(u32x2*)(out + off + 8) = o1;
    };
    u32x4 v[NCH]; u32x2 xa, xb;
    reduce1(wid, lane, v, xa, xb);
    if (wid >= 6) {
      const unsigned u = (wid - 6) * 64 + lane, ln = u >> 1, g = u & 1, db = 16 + (wt >> 2);
      const unsigned short* src = part + ((size_t)(tb * 18 + db) * 2 + hf2) * 8192 + ln * 8 + g * 4;
      u32x2 v2[NCH];
#pragma unroll
      for (int cc = 0; cc < NCH; ++cc) asm volatile("global_load_dwordx2 %0, %1, off sc0 sc1" : "=v"(v2[cc]) : "v"(src + cc * 512) : "memory");
      const size_t off = (size_t)(tb * 32 + (ln & 31)) * D_ + db * 32 + hf2 * 16 + 4 * (ln >> 5) + 8 * g;
      const u32x2 xa2 = *(const u32x2*)(x + off);
      WAITV(v); WAITV(v2);
      SBAR();
      finish1(wid, lane, v, xa, xb);
      SBAR();
      f32x2 s0 = {lo_f(xa2[0]), hi_f(xa2[0])}, s1 = {lo_f(xa2[1]), hi_f(xa2[1])}, t0 = {0.f, 0.f}, t1 = {0.f, 0.f};
#pragma unroll
      for (int cc = 0; cc < NCH; ++cc) { t0 += f32x2{lo_f(v2[cc][0]), hi_f(v2[cc][0])}; t1 += f32x2{lo_f(v2[cc][1]), hi_f(v2[cc][1])}; }
      *(u32x2*)(out + off) = u32x2{cvtpk(s0[0] + t0[0], s0[1] + t0[1]), cvtpk(s1[0] + t1[0], s1[1] + t1[1])};
    } else { WAITV(v); finish1(wid, lane, v, xa, xb); }
  }
  STAMP(10);
  if (blockIdx.x == 0 && wid == 0) {
#pragma unroll 1
    for (int i = 0; i < 4; ++i) fl_wait(FL(FL_B + i * 64 + lane), tag);
    if (lane == 0) fl_store(FL(0), tag);
  }
}

extern "C" void kernel_launch(void* const* d_in, const int* in_sizes, int n_in, void* d_out, int out_size, void* d_ws, size_t ws_size, hipStream_t stream) {
  const unsigned short* x = (const unsigned short*)d_in[0];
  const char* wqkv = (const char*)d_in[1];
  const char* wo = (const char*)d_in[2];
  const char* wg = (const char*)d_in[4]; const char* wu = (const char*)d_in[5]; const char* wd = (const char*)d_in[6];
  char* ws = (char*)d_ws;
  u64* fl64 = (u64*)ws;
  char* halo = ws + (1u << 20);
  char* xf = ws + (4u << 20);
  unsigned short* part = (unsigned short*)(ws + (8u << 20));
  char* wqp = ws + (48u << 20);
  (void)hipFuncSetAttribute((const void*)k_main, hipFuncAttributeMaxDynamicSharedMemorySize, LDS_BYTES);
  k_main<<<256, 512, LDS_BYTES, stream>>>(x, wqkv, wg, wu, wd, wo, part, halo, fl64, xf, wqp, (unsigned short*)d_out);
}
```

```cpp
#include <hip/hip_runtime.h>
#include <hip/hip_bf16.h>
#include <stdint.h>
#include <utility>
#include <type_traits>

using bf16x8 = __attribute__((ext_vector_type(8))) short;
using s16x4  = __attribute__((ext_vector_type(4))) short;
using f32x16 = __attribute__((ext_vector_type(16))) float;
using f32x2  = __attribute__((ext_vector_type(2))) float;
using u32x4  = __attribute__((ext_vector_type(4))) unsigned;
using u32x2  = __attribute__((ext_vector_type(2))) unsigned;
#define LAS __attribute__((address_space(3)))
typedef LAS const char* lds_cptr;
typedef LAS char* lds_ptr;
typedef short v4i16_t __attribute__((ext_vector_type(4)));
typedef __bf16 bf16x2_t __attribute__((ext_vector_type(2)));
#define DI __device__ __forceinline__
#define SBAR() __builtin_amdgcn_sched_barrier(0)

constexpr int S_ = 2048, D_ = 576, F_ = 4608;
constexpr int NKS = 36;
constexpr int NCH = 16;
constexpr int SL = 26624;
constexpr int NSL = 6;
constexpr int LDS_BYTES = 163840;
constexpr unsigned CODE_PF_BYTES = 53 * 1024;
constexpr int QP = 232;

DI unsigned cvtpk(float lo, float hi) { f32x2 v = {lo, hi}; bf16x2_t b = __builtin_convertvector(v, bf16x2_t); return __builtin_bit_cast(unsigned, b); }
DI float rbf(float f) { return __uint_as_float(cvtpk(f, 0.f) << 16); }
DI float lo_f(unsigned p) { return __uint_as_float(p << 16); }
DI float hi_f(unsigned p) { return __uint_as_float(p & 0xffff0000u); }
DI float dot2(unsigned a, unsigned b, float c) { return __builtin_amdgcn_fdot2_f32_bf16(__builtin_bit_cast(bf16x2_t, a), __builtin_bit_cast(bf16x2_t, b), c, false); }
DI s16x4 vtr(lds_cptr p) { return __builtin_bit_cast(s16x4, __builtin_amdgcn_ds_read_tr16_b64_v4i16((LAS v4i16_t*)p)); }
DI bf16x8 trfrag(lds_cptr p, int o0, int o1) { s16x4 a = vtr(p + o0); SBAR(); s16x4 b = vtr(p + o1); return __builtin_shufflevector(a, b, 0, 1, 2, 3, 4, 5, 6, 7); }
DI void glds16s(unsigned voff, const void* sbase, unsigned lds_base) {
  asm volatile("s_mov_b32 m0, %2\n\ts_nop 0\n\tglobal_load_lds_dwordx4 %0, %1" :: "v"(voff), "s"(sbase), "s"(lds_base) : "memory", "m0");
}
#define PSTORE(p, v) asm volatile("global_store_dwordx4 %0, %1, off sc0 sc1\n\ts_nop 1" :: "v"(p), "v"(v) : "memory")
#define MFMA(a, b, c) __builtin_amdgcn_mfma_f32_32x32x16_bf16((a), (b), (c), 0, 0, 0)
template <int N> DI void wait_bar() { asm volatile("s_waitcnt vmcnt(%0)\n\ts_barrier" :: "i"(N) : "memory"); }
template <int N> DI void wait_bar_l() { asm volatile("s_waitcnt vmcnt(%0) lgkmcnt(0)\n\ts_barrier" :: "i"(N) : "memory"); }
DI void bar_l() { asm volatile("s_waitcnt lgkmcnt(0)\n\ts_barrier" ::: "memory"); }
template <int C> DI lds_cptr vaddc(unsigned v) { unsigned r; if (C == 0) r = v; else asm volatile("v_add_u32_e32 %0, %2, %1" : "=v"(r) : "v"(v), "i"(C)); return (lds_cptr)(size_t)r; }
DI float relu1(float x) { float r; asm("v_max_f32_e32 %0, 0, %1" : "=v"(r) : "v"(x)); return r; }
DI unsigned fresh_lane() { unsigned l; asm volatile("v_mbcnt_lo_u32_b32 %0, -1, 0\n\tv_mbcnt_hi_u32_b32 %0, -1, %0" : "=v"(l)); return l; }

#define STAMP(i)

typedef unsigned long long u64;
#define FL(i) (fl64 + (size_t)(i) * 16)
constexpr int FL_LN = 1, FL_HALO = 257, FL_A = 513, FL_B = 769;
DI u64 fl_load(const u64* p) { return __hip_atomic_load(p, __ATOMIC_RELAXED, __HIP_MEMORY_SCOPE_AGENT); }
DI void fl_store(u64* p, u64 v) { __hip_atomic_store(p, v, __ATOMIC_RELAXED, __HIP_MEMORY_SCOPE_AGENT); }
DI void fl_wait(const u64* p, u64 tag) { unsigned spins = 0; while (!__all(fl_load(p) == tag) && ++spins < (1u << 22)) __builtin_amdgcn_s_sleep(1); asm volatile("" ::: "memory"); }
#define ST128(p, v) asm volatile("global_store_dwordx4 %0, %1, off sc0 sc1\n\ts_nop 1" :: "v"(p), "v"(v) : "memory")
#define ST64(p, v) asm volatile("global_store_dwordx2 %0, %1, off sc0 sc1" :: "v"(p), "v"(v) : "memory")

template <class F, int... I> DI void for_each_int(F&& f, std::integer_sequence<int, I...>) { (f(std::integral_constant<int, I>{}), ...); }
#define PFST 4
#define NPF 1
struct Ptrs { const char* wg; const char* wu; const char* xm; const char* wq; };

DI void dma_piece(const Ptrs& P, unsigned p, unsigned hh, unsigned ldsdst) {
  const unsigned l = fresh_lane();
  if (p < 18) {
    const unsigned mat = p >= 9 ? 1u : 0u, pj = p - 9 * mat;
    const unsigned g = l + 64 * pj, row = (g * 1821u) >> 16;
    glds16s(row * 8640u + g * 16u, (mat ? P.wu : P.wg) + (size_t)hh * (16 * F_ * 2), ldsdst + p * 1024);
  } else if (p < 22) {
    glds16s(l * 16u, P.xm + (size_t)((p - 18) * NKS + hh) * 1024, ldsdst + p * 1024);
  } else {
    glds16s(l * 16u, P.wq + (size_t)hh * 4096 + (p - 22) * 1024, ldsdst + p * 1024);
  }
}

template <int FH, class F, class G> DI void phase1(F&& tail_dma, G&& l2_prefetch, const Ptrs& P, const unsigned w4, const unsigned l0, lds_cptr ldsb, const unsigned rdA, const unsigned rdX, const unsigned (&rdQ)[3], f32x16 (&acc)[11]) {
  constexpr int LA = 5, RING = 6;
  auto issue_odd = [&](unsigned hh, unsigned so) {
#pragma unroll
    for (int k = 0; k < 7; ++k) if (k < 6 || w4 < 2) dma_piece(P, w4 + 4 * k, hh, l0 + so);
  };
  auto issue_evenB = [&](unsigned hh, unsigned so) {
    if (w4 < 2) dma_piece(P, 20 + w4, hh, l0 + so); else { dma_piece(P, 18 + 2 * w4, hh, l0 + so); dma_piece(P, 19 + 2 * w4, hh, l0 + so); }
  };
  auto issue_evenA = [&](unsigned hh, unsigned so) {
#pragma unroll
    for (int k = 0; k < 5; ++k) dma_piece(P, w4 + 4 * k, hh, l0 + so);
  };
  bf16x8 fr[RING]; bf16x8 xq[2];
  constexpr int NGU = FH ? 8 : 10;
  lds_cptr aG = nullptr, aQ[3] = {nullptr, nullptr, nullptr};
  const unsigned rA = l0 + rdA, rX = l0 + rdX, rQ0 = l0 + rdQ[0], rQ1 = l0 + rdQ[1], rQ2 = l0 + rdQ[2];
#define SET_SLOT(slot) do { aG = vaddc<(slot) * SL>(rA); aQ[0] = vaddc<(slot) * SL>(rQ0); if (FH) { aQ[1] = vaddc<(slot) * SL>(rQ1); aQ[2] = vaddc<(slot) * SL>(rQ2); } } while (0)
#define RD(dst, n) do { constexpr int nn_ = (n) % 66, slot_ = nn_ / 11, t_ = nn_ % 11; if (t_ == 0) SET_SLOT(slot_); \
    if (t_ < NGU) dst = trfrag(aG, (t_ & 1) * 9216 + (t_ >> 1) * 64, (t_ & 1) * 9216 + (t_ >> 1) * 64 + 4 * 576); else dst = trfrag(aQ[t_ < NGU ? 0 : t_ - NGU], 0, 4 * 256); } while (0)
#define RDX(slot) (*(const LAS bf16x8*)vaddc<(slot) * SL>(rX))
  xq[0] = RDX(0);
  SBAR();
  RD(fr[0], 0); SBAR(); RD(fr[1], 1); SBAR(); RD(fr[2], 2); SBAR(); RD(fr[3], 3); SBAR(); RD(fr[4], 4); SBAR();
#pragma unroll 1
  for (int it = 0; it < 6; ++it) {
    auto step = [&](auto n_) {
      constexpr int n = decltype(n_)::value;
      constexpr int S3 = n / 22, m = n % 22;
      const int st = 3 * it + S3;
      if (m == 22 - LA) {
        if (!FH && st <= 15) issue_evenA(2 * st + 4, ((2 * S3 + 4) % 6) * SL);
        if (st < 17) {
          if (st == 16) wait_bar<0>();
          else if (FH) { if (st == PFST + 1) { if (w4 < 2) wait_bar<1 + NPF>(); else wait_bar<2 + NPF>(); } else { if (w4 < 2) wait_bar<1>(); else wait_bar<2>(); } }
          else wait_bar<5>();
        }
        xq[0] = RDX((2 * S3 + 2) % 6);
        if (FH) { if (st <= 15) issue_odd(2 * st + 5, ((2 * S3 + 5) % 6) * SL); if (st <= 14) issue_evenB(2 * st + 6, ((2 * S3 + 6) % 6) * SL); }
        if (S3 == 1 && st == 16) tail_dma();
        if (FH && st == PFST) l2_prefetch();
      }
      if (m == 11 - LA) xq[1] = RDX(2 * S3 + 1);
      const bf16x8 xcur = xq[m / 11];
      RD(fr[(n + LA) % RING], n + LA);
      acc[m % 11] = MFMA(fr[n % RING], xcur, acc[m % 11]);
      SBAR();
    };
    for_each_int(step, std::make_integer_sequence<int, 66>{});
  }
#undef RD
#undef RDX
#undef SET_SLOT
}

__global__ __launch_bounds__(512, 2) void k_main(const unsigned short* __restrict__ x, const char* __restrict__ wqkv, const char* __restrict__ wg, const char* __restrict__ wu,
                                                 const char* __restrict__ wd, const char* __restrict__ wo, unsigned short* __restrict__ part,
                                                 char* __restrict__ halo, u64* __restrict__ fl64, char* __restrict__ xf, char* __restrict__ wqp,
                                                 unsigned short* __restrict__ out) {
  extern __shared__ __attribute__((aligned(16))) char lds[];
  STAMP(0);
  const int tid = threadIdx.x, lane = tid & 63;
  const unsigned wid = __builtin_amdgcn_readfirstlane(tid >> 6);
  const unsigned tg = wid & 3, fh = wid >> 2;
  const unsigned bx = blockIdx.x, xcd = bx & 7, kk = bx >> 3;
  const unsigned c = xcd * 2 + (kk & 1), mt = kk >> 1;
  const unsigned l0 = (unsigned)(uintptr_t)lds;
  const lds_cptr ldsb = (lds_cptr)lds;
  Ptrs P;
  P.wg = wg + 576 * c; P.wu = wu + 576 * c; P.xm = xf + (size_t)(mt * 4) * NKS * 1024; P.wq = wqp + (size_t)c * (576 * 256);
  const unsigned h = lane >> 5, blk16 = (lane >> 4) & 1, q4 = (lane & 15) >> 2, p4 = lane & 3;
  const unsigned rdA = (8 * h + q4) * 576 + blk16 * 32 + p4 * 8 + fh * 5 * 64;
  const unsigned rdX = 18432 + tg * 1024 + lane * 16;
  unsigned rdQ[3];
#pragma unroll
  for (int i = 0; i < 3; ++i) { const unsigned t = fh ? i + 1 : 0; rdQ[i] = 22528 + (8 * h + q4) * 256 + ((t ^ q4) * 64) + blk16 * 32 + p4 * 8; }
  const unsigned lrow = lane >> 3, ch2 = (lane & 7) ^ (((lrow >> 1) & 1) << 2);
  const unsigned voff2 = lrow * (D_ * 2) + ch2 * 16;
  auto dma2j = [&](int q, unsigned j) {
    const int hh = (2 * q) % 6;
    const unsigned so = j >= 22 ? ((hh + 1) % 6) * SL + (j - 22) * 1024 : hh * SL + j * 1024;
    if (j < 36) glds16s(voff2, wd + (size_t)(288 * c + 8 * j) * (D_ * 2) + q * 128, l0 + so);
    else { const unsigned l = fresh_lane(), lr = l >> 3; unsigned wr = 36 * c + 8 * (j - 36) + lr; if (wr > 575) wr = 575;
      glds16s(wr * (D_ * 2) + (((l & 7) ^ (((lr >> 1) & 1) << 2)) * 16), wo + q * 128, l0 + so); }
  };
  auto dma2_A = [&](int q) {
#pragma unroll
    for (int k = 0; k < 5; ++k) dma2j(q, tg + 4 * k);
  };
  auto dma2_Bodd = [&](int q) {
#pragma unroll
    for (int k = 0; k < 5; ++k) dma2j(q, 22 + tg + 4 * k);
  };
  auto dma2_Beven = [&](int q) { if (tg < 2) dma2j(q, 20 + tg); };
  auto dma2_all = [&](int q) { if (fh) { dma2_Beven(q); dma2_Bodd(q); } else dma2_A(q); };
  auto l2_prefetch = [&]() {
    unsigned idx = tg * 64 + fresh_lane();
    const char* a;
    if (idx < 162) a = wd + (size_t)(288 * c + 18 * mt) * (D_ * 2) + idx * 128;
    else { unsigned k = idx - 162; if (k > 20) k = 20; k += mt * 21; if (k > 323) k = 323; a = wo + (size_t)(36 * c) * (D_ * 2) + k * 128; }
    asm volatile("s_mov_b32 m0, %1\n\ts_nop 0\n\tglobal_load_lds_dword %0, off" :: "v"(a), "s"(l0 + NSL * SL + wid * 256) : "memory", "m0");
  };
  if (wid == 4) {
    u64 pc; asm volatile("s_getpc_b64 %0" : "=s"(pc));
    unsigned off = (kk * 14 + (lane < 14 ? lane : 13)) * 128; if (off > CODE_PF_BYTES) off = CODE_PF_BYTES;
    asm volatile("s_mov_b32 m0, %1\n\ts_nop 0\n\tglobal_load_lds_dword %0, off" :: "v"((const char*)(pc & ~127ull) + off), "s"(l0 + NSL * SL + wid * 256) : "memory", "m0");
  }
  const u64 epoch = fl_load(FL(0));
  if (fh) {
#pragma unroll 1
    for (int hh = 0; hh < 4; ++hh)
#pragma unroll
      for (int k = 0; k < 5; ++k) if (tg + 4 * k < 18) dma_piece(P, tg + 4 * k, hh, l0 + hh * SL);
  } else if (wid == 0) {
    const int j = lane & 7, t = 128 * mt + 8 * c + (lane >> 3);
    const u32x4* src = (const u32x4*)(x + (size_t)t * D_) + j;
    u32x4 v[9];
#pragma unroll
    for (int i = 0; i < 9; ++i) v[i] = src[8 * i];
    float s = 0, q = 0;
#pragma unroll
    for (int i = 0; i < 9; ++i)
#pragma unroll
      for (int e = 0; e < 4; ++e) { s = dot2(v[i][e], 0x3f803f80u, s); q = dot2(v[i][e], v[i][e], q); }
#pragma unroll
    for (int o = 1; o < 8; o <<= 1) { s += __shfl_xor(s, o); q += __shfl_xor(q, o); }
    const float m = s * (1.f / D_);
    const float var = fmaxf(q * (1.f / D_) - m * m, 0.f);
    const float mb = rbf(m), vb = rbf(var);
    const float ri = 1.f / rbf(sqrtf(rbf(vb + 1e-5f)));
    const f32x2 mb2 = {mb, mb}, ri2 = {ri, ri};
    char* gdst = xf + (size_t)((t >> 5) * NKS) * 1024 + (t & 31) * 16 + (j & 1) * 512 + (j >> 1) * 1024;
#pragma unroll
    for (int i = 0; i < 9; ++i) {
      u32x4 o;
#pragma unroll
      for (int e = 0; e < 4; ++e) { f32x2 a = {lo_f(v[i][e]), hi_f(v[i][e])}; a -= mb2; const unsigned d = cvtpk(a[0], a[1]); f32x2 b = {lo_f(d), hi_f(d)}; b *= ri2; o[e] = cvtpk(b[0], b[1]); }
      ST128(gdst + i * 4096, o);
    }
    asm volatile("s_waitcnt vmcnt(0)" ::: "memory");
  } else {
    u32x2 w[6];
#pragma unroll
    for (int i = 0; i < 6; ++i) {
      const unsigned id = ((wid - 1) * 6 + i) * 64 + lane, row = 36 * mt + (id >> 5), qd = id & 31, pt = qd / 9;
      w[i] = u32x2{0u, 0u};
      if (qd < 27) w[i] = *(const u32x2*)(wqkv + (size_t)row * 3456 + pt * 1152 + 72 * c + (qd - 9 * pt) * 8);
    }
#pragma unroll
    for (int i = 0; i < 6; ++i) {
      const unsigned id = ((wid - 1) * 6 + i) * 64 + lane, row = 36 * mt + (id >> 5), qd = id & 31;
      ST64(wqp + (size_t)(c * 576 + row) * 256 + (((qd >> 3) ^ (row & 3)) * 64) + (qd & 7) * 8, w[i]);
    }
    asm volatile("s_waitcnt vmcnt(0)" ::: "memory");
  }
  const u64 tag = (((u64)(unsigned)__builtin_amdgcn_readfirstlane((unsigned)(epoch >> 32)) << 32) | (u64)(unsigned)__builtin_amdgcn_readfirstlane((unsigned)epoch)) + 1ull;
  STAMP(11);
  __builtin_amdgcn_s_barrier();
  STAMP(4);
  if (wid == 0) {
    if (lane == 0) fl_store(FL(FL_LN + mt * 16 + c), tag);
    fl_wait(FL(FL_LN + ((lane & 16) ? (lane & 15) * 16 + c : mt * 16 + (lane & 15))), tag);
    STAMP(12);
  }
  __builtin_amdgcn_s_barrier();
  STAMP(13);
#pragma unroll
  for (int k = 0; k < 5; ++k) { const unsigned i = wid + 8 * k; if (i < 32) dma_piece(P, 18 + (i & 7), i >> 3, l0 + (i >> 3) * SL); else if (i < 38) dma_piece(P, 20 + (i - 32), 4, l0 + 4 * SL); }
  wait_bar<0>();
  STAMP(1);
  const f32x16 zero = {};
  f32x16 acc[11];
#pragma unroll
  for (int i = 0; i < 11; ++i) acc[i] = zero;
  if (fh) phase1<1>([&]() { dma2_all(0); }, l2_prefetch, P, tg, l0, ldsb, rdA, rdX, rdQ, acc); else phase1<0>([&]() { dma2_all(0); }, l2_prefetch, P, tg, l0, ldsb, rdA, rdX, rdQ, acc);
  const unsigned tag_lo = (unsigned)tag, tag_hi = (unsigned)(tag >> 32);
  if (wid == 7) {
    char* hb = halo + (size_t)(mt * 16 + c) * 8192 + ((lane & 15) * 288);
    if (lane & 16) {
#pragma unroll
      for (int t = 1; t < 4; ++t)
#pragma unroll
        for (int g = 0; g < 4; ++g) {
          const int c0 = 32 * t + 8 * g;
          if (c0 + 4 >= 36 && c0 < 108) {
            const u32x4 w = {cvtpk(acc[7 + t][4 * g], acc[7 + t][4 * g + 1]), cvtpk(acc[7 + t][4 * g + 2], acc[7 + t][4 * g + 3]), tag_lo, tag_hi};
            const int cc = c0 + 4 * (int)h;
            if (cc >= 36 && cc < 108) asm volatile("global_store_dwordx4 %0, %1, off sc0 sc1\n\ts_nop 1" :: "v"(hb + (cc - 36) * 4), "v"(w) : "memory");
          }
        }
    }
  }
  u32x4 own[10];
#pragma unroll
  for (int b = 0; b < 5; ++b) if (b < 4 || fh == 0)
#pragma unroll
    for (int i = 0; i < 8; ++i) {
      f32x2 g2 = {relu1(acc[2 * b][2 * i]), relu1(acc[2 * b][2 * i + 1])};
      g2 *= f32x2{acc[2 * b + 1][2 * i], acc[2 * b + 1][2 * i + 1]};
      own[2 * b + (i >> 2)][i & 3] = cvtpk(g2[0], g2[1]);
    }
  bar_l();
  STAMP(2);
  constexpr int HX = 2 * SL, QK = HX + 73728;
  const unsigned hxo = HX + tg * 18432 + lane * 16;
  {
    const lds_ptr hx = (lds_ptr)lds + hxo + fh * 10240;
#pragma unroll
    for (int i = 0; i < 10; ++i) if (i < 8 || fh == 0) *(LAS u32x4*)(hx + i * 1024) = own[i];
    lds_ptr d = (lds_ptr)lds + QK + (16 + tg * 32 + (lane & 31)) * QP + 8 * h;
    auto put = [&](const f32x16& a, int t) {
#pragma unroll
      for (int g = 0; g < 4; ++g) if (32 * t + 8 * g < 108) {
        const u32x2 w = {cvtpk(a[4 * g], a[4 * g + 1]), cvtpk(a[4 * g + 2], a[4 * g + 3])};
        if (32 * t + 8 * g + 4 < 108 || h == 0) *(LAS u32x2*)(d + (32 * t + 8 * g) * 2) = w;
      }
    };
    if (fh) { put(acc[8], 1); put(acc[9], 2); put(acc[10], 3); } else put(acc[10], 0);
  }
  if (mt > 0 && wid == 0) {
    const char* hb = halo + (size_t)((mt - 1) * 16 + c) * 8192;
    u32x4 v[5]; unsigned spins = 0;
    for (;;) {
#pragma unroll
      for (int i = 0; i < 5; ++i) { unsigned id = lane + 64 * i; if (id > 287) id = 287; asm volatile("global_load_dwordx4 %0, %1, off sc0 sc1" : "=v"(v[i]) : "v"(hb + id * 16) : "memory"); }
      asm volatile("s_waitcnt vmcnt(0)" : "+v"(v[0]), "+v"(v[1]), "+v"(v[2]), "+v"(v[3]), "+v"(v[4]) :: "memory");
      bool ok = true;
#pragma unroll
      for (int i = 0; i < 5; ++i) ok = ok && v[i][2] == tag_lo && v[i][3] == tag_hi;
      if (__all(ok) || ++spins >= (1u << 20)) break;
      __builtin_amdgcn_s_sleep(1);
    }
#pragma unroll
    for (int i = 0; i < 5; ++i) {
      const unsigned id = lane + 64 * i;
      if (id < 288) { const unsigned tk = id / 18, ck = id - tk * 18; *(LAS u32x2*)((lds_ptr)lds + QK + tk * QP + 72 + ck * 8) = u32x2{v[i][0], v[i][1]}; }
    }
  }
  if (mt == 0 && wid == 0) {
#pragma unroll
    for (int i = 0; i < 8; ++i) { const unsigned id = lane + 64 * i; if (id < 16 * (QP / 8)) *(LAS u32x2*)((lds_ptr)lds + QK + id * 8) = u32x2{0u, 0u}; }
  }
  bar_l();
  STAMP(3);
  bf16x8 hf[21];
#pragma unroll
  for (int s = 0; s < 18; ++s) hf[s] = *(const LAS bf16x8*)(ldsb + hxo + s * 1024);
  STAMP(5);
  float r10[10];
  const int tok = (tid & 31) + 32 * ((tid >> 6) & 3), wp = (tid >> 5) & 1; const int head = tid >> 8;
  {
    const int t = 128 * mt + tok, slot = 16 + tok;
    const int ox = head * 40, oy = head ? 36 : 32;
    const lds_cptr qp = ldsb + QK + slot * QP;
    unsigned qv[9];
#pragma unroll
    for (int i = 0; i < 4; ++i) { const u32x2 u = *(const LAS u32x2*)(qp + ox + 8 * i); qv[2 * i] = u[0]; qv[2 * i + 1] = u[1]; }
    qv[8] = *(const LAS unsigned*)(qp + oy);
    float lg[4];
#pragma unroll
    for (int i4 = 0; i4 < 4; ++i4) {
      const int w = 4 * wp + i4;
      const lds_cptr kp = qp - 2 * w * QP + 72;
      float a = 0;
#pragma unroll
      for (int i = 0; i < 4; ++i) { const u32x2 u = *(const LAS u32x2*)(kp + ox + 8 * i); a = dot2(qv[2 * i], u[0], a); a = dot2(qv[2 * i + 1], u[1], a); }
      a = dot2(qv[8], *(const LAS unsigned*)(kp + oy), a);
      lg[i4] = (t - 2 * w >= 0) ? rbf(rbf(a) * 0.23570226039551584f) : -1e9f;
    }
    float mx = fmaxf(fmaxf(lg[0], lg[1]), fmaxf(lg[2], lg[3]));
    mx = fmaxf(mx, __shfl_xor(mx, 32));
    float sum = 0;
#pragma unroll
    for (int i = 0; i < 4; ++i) { lg[i] = __expf(lg[i] - mx); sum += lg[i]; }
    sum += __shfl_xor(sum, 32);
    const float inv = 1.f / sum;
    f32x2 o[9];
#pragma unroll
    for (int i = 0; i < 9; ++i) o[i] = f32x2{0.f, 0.f};
#pragma unroll
    for (int i4 = 0; i4 < 4; ++i4) {
      const int w = 4 * wp + i4;
      const float p = rbf(lg[i4] * inv); const f32x2 p2 = {p, p};
      const lds_cptr vp = qp - 2 * w * QP + 144;
#pragma unroll
      for (int i = 0; i < 4; ++i) { const u32x2 u = *(const LAS u32x2*)(vp + ox + 8 * i);
        o[2 * i] += p2 * f32x2{lo_f(u[0]), hi_f(u[0])}; o[2 * i + 1] += p2 * f32x2{lo_f(u[1]), hi_f(u[1])}; }
      const unsigned y = *(const LAS unsigned*)(vp + oy);
      o[8] += p2 * f32x2{lo_f(y), hi_f(y)};
    }
#pragma unroll
    for (int i = 0; i < 5; ++i) {
      f32x2 a = (i < 4) ? o[i] : o[8], b = (i < 4) ? o[4 + i] : f32x2{0.f, 0.f};
      asm volatile("" : "+v"(a), "+v"(b));
      const f32x2 send = wp ? a : b, keep = wp ? b : a;
      r10[2 * i] = keep[0] + __shfl_xor(send[0], 32); r10[2 * i + 1] = keep[1] + __shfl_xor(send[1], 32);
    }
  }
  bar_l();
  {
    lds_ptr op = (lds_ptr)lds + HX + tok * 104 + head * 36;
#pragma unroll
    for (int i = 0; i < 4; ++i) *(LAS unsigned*)(op + 4 * (head + 4 * wp + i)) = cvtpk(r10[2 * i], r10[2 * i + 1]);
    if (wp == 0) *(LAS unsigned*)(op + (head ? 0 : 32)) = cvtpk(r10[8], r10[9]);
    if (head == 1 && wp == 1) { lds_ptr z = (lds_ptr)lds + HX + tok * 104 + 72; *(LAS u32x2*)z = u32x2{0u, 0u}; *(LAS u32x2*)(z + 8) = u32x2{0u, 0u}; *(LAS u32x2*)(z + 16) = u32x2{0u, 0u}; }
  }
  bar_l();
  STAMP(6);
#pragma unroll
  for (int s = 0; s < 3; ++s) {
    const lds_cptr p = ldsb + HX + (tg * 32 + (lane & 31)) * 104 + (16 * s + 4 * h) * 2;
    const u32x2 a = *(const LAS u32x2*)p, b = *(const LAS u32x2*)(p + 16);
    hf[18 + s] = __builtin_bit_cast(bf16x8, u32x4{a[0], a[1], b[0], b[1]});
  }
  wait_bar_l<0>();
  dma2_all(1); dma2_all(2);
  STAMP(7);
  const unsigned colb = (((4 * fh + 2 * blk16 + (p4 >> 1)) ^ ((q4 >> 1) << 2)) * 16) + 8 * (p4 & 1);
  const lds_cptr rb2 = ldsb + (4 * h + q4) * 128 + colb;
  unsigned short* pp = part + (((size_t)(mt * 4 + tg) * 18 + fh) * 32 + c) * 512 + lane * 8;
  {
    constexpr int LA = 6, RING = 7, CNT = 21;
    bf16x8 fr[RING];
    auto rd2 = [&](int q, int n) -> bf16x8 {
      const int hh = (2 * q + (n >= 11 ? 1 : 0)) % 6, nn = n >= 11 ? n - 11 : n;
      return trfrag(rb2 + hh * SL, nn * 2048, nn * 2048 + 1024);
    };
#pragma unroll
    for (int n = 0; n < LA; ++n) { fr[n] = rd2(0, n); SBAR(); }
    f32x16 acc2[2];
#pragma unroll
    for (int q = 0; q < 9; ++q) {
#pragma unroll
      for (int n = 0; n < CNT; ++n) {
        if (n == CNT - LA && q < 8) {
          if (!fh && q >= 1 && q + 2 < 9) dma2_A(q + 2);
          if (fh) {
            if (q == 0) { if (tg < 2) wait_bar<6>(); else wait_bar<5>(); }
            else if (q < 7) { if (tg < 2) wait_bar<3>(); else wait_bar<2>(); }
            else wait_bar<2>();
          } else {
            if (q == 0) wait_bar<5>(); else if (q < 7) wait_bar<7>(); else wait_bar<2>();
          }
          if (fh) { if (q >= 1 && q + 2 < 9) dma2_Bodd(q + 2); if (q + 3 < 9) dma2_Beven(q + 3); }
          if (q == 7 && tid == 64) fl_store(FL(FL_A + mt * 16 + c), tag);
        }
        if (n + LA < CNT) fr[(n + LA) % RING] = rd2(q, n + LA);
        else if (q < 8) fr[(n + LA) % RING] = rd2(q + 1, n + LA - CNT);
        acc2[q & 1] = MFMA(fr[n % RING], hf[n], n == 0 ? zero : acc2[q & 1]);
        if (n == 3 && q > 0) {
          u32x4 o0, o1;
#pragma unroll
          for (int i = 0; i < 4; ++i) { o0[i] = cvtpk(acc2[(q - 1) & 1][2 * i], acc2[(q - 1) & 1][2 * i + 1]); o1[i] = cvtpk(acc2[(q - 1) & 1][8 + 2 * i], acc2[(q - 1) & 1][8 + 2 * i + 1]); }
          PSTORE(pp + (q - 1) * 32768, o0); PSTORE(pp + (q - 1) * 32768 + 8192, o1);
        }
        SBAR();
      }
    }
    u32x4 o0, o1;
#pragma unroll
    for (int i = 0; i < 4; ++i) { o0[i] = cvtpk(acc2[0][2 * i], acc2[0][2 * i + 1]); o1[i] = cvtpk(acc2[0][8 + 2 * i], acc2[0][8 + 2 * i + 1]); }
    PSTORE(pp + 8 * 32768, o0); PSTORE(pp + 8 * 32768 + 8192, o1);
  }
  STAMP(8);
  asm volatile("s_waitcnt vmcnt(0)" ::: "memory");
  __builtin_amdgcn_s_barrier();
  if (tid == 0) fl_store(FL(FL_B + mt * 16 + c), tag);
  {
    fl_wait(FL((wid < 6 ? FL_A : FL_B) + mt * 16 + (lane & 15)), tag);
  }
  STAMP(9);
  {
    const unsigned wt = c >> 1, tb = mt * 4 + (wt & 3), hf2 = c & 1;
    auto reduce1 = [&](unsigned r, unsigned ln, u32x4 (&v)[NCH], u32x2& xa, u32x2& xb) {
      const unsigned db = 2 * r + (wt >> 2);
      const unsigned short* src = part + ((size_t)(tb * 18 + db) * 2 + hf2) * 8192 + ln * 8;
#pragma unroll
      for (int cc = 0; cc < NCH; ++cc) asm volatile("global_load_dwordx4 %0, %1, off sc0 sc1" : "=v"(v[cc]) : "v"(src + cc * 512) : "memory");
      const size_t off = (size_t)(tb * 32 + (ln & 31)) * D_ + db * 32 + hf2 * 16 + 4 * (ln >> 5);
      xa = *(const u32x2*)(x + off); xb = *(const u32x2*)(x + off + 8);
    };
#define WAITV(v) asm volatile("s_waitcnt vmcnt(0)" : "+v"(v[0]), "+v"(v[1]), "+v"(v[2]), "+v"(v[3]), "+v"(v[4]), "+v"(v[5]), "+v"(v[6]), "+v"(v[7]), "+v"(v[8]), "+v"(v[9]), "+v"(v[10]), "+v"(v[11]), "+v"(v[12]), "+v"(v[13]), "+v"(v[14]), "+v"(v[15]) :: "memory")
    auto finish1 = [&](unsigned r, unsigned ln, const u32x4 (&v)[NCH], const u32x2& xa, const u32x2& xb) {
      const unsigned db = 2 * r + (wt >> 2);
      const size_t off = (size_t)(tb * 32 + (ln & 31)) * D_ + db * 32 + hf2 * 16 + 4 * (ln >> 5);
      f32x2 a[4] = {{lo_f(xa[0]), hi_f(xa[0])}, {lo_f(xa[1]), hi_f(xa[1])}, {lo_f(xb[0]), hi_f(xb[0])}, {lo_f(xb[1]), hi_f(xb[1])}};
      f32x2 sm[4] = {{0.f, 0.f}, {0.f, 0.f}, {0.f, 0.f}, {0.f, 0.f}};
#pragma unroll
      for (int cc = 0; cc < NCH; ++cc) {
#pragma unroll
        for (int i = 0; i < 4; ++i) sm[i] += f32x2{lo_f(v[cc][i]), hi_f(v[cc][i])};
        if ((cc & 3) == 3) SBAR(); }
      u32x2 o0 = {cvtpk(a[0][0] + sm[0][0], a[0][1] + sm[0][1]), cvtpk(a[1][0] + sm[1][0], a[1][1] + sm[1][1])};
      u32x2 o1 = {cvtpk(a[2][0] + sm[2][0], a[2][1] + sm[2][1]), cvtpk(a[3][0] + sm[3][0], a[3][1] + sm[3][1])};
      *(u32x2*)(out + off) = o0; *(u32x2*)(out + off + 8) = o1;
    };
    u32x4 v[NCH]; u32x2 xa, xb;
    reduce1(wid, lane, v, xa, xb);
    if (wid >= 6) {
      const unsigned u = (wid - 6) * 64 + lane, ln = u >> 1, g = u & 1, db = 16 + (wt >> 2);
      const unsigned short* src = part + ((size_t)(tb * 18 + db) * 2 + hf2) * 8192 + ln * 8 + g * 4;
      u32x2 v2[NCH];
#pragma unroll
      for (int cc = 0; cc < NCH; ++cc) asm volatile("global_load_dwordx2 %0, %1, off sc0 sc1" : "=v"(v2[cc]) : "v"(src + cc * 512) : "memory");
      const size_t off = (size_t)(tb * 32 + (ln & 31)) * D_ + db * 32 + hf2 * 16 + 4 * (ln >> 5) + 8 * g;
      const u32x2 xa2 = *(const u32x2*)(x + off);
      WAITV(v); WAITV(v2);
      SBAR();
      finish1(wid, lane, v, xa, xb);
      SBAR();
      f32x2 s0 = {lo_f(xa2[0]), hi_f(xa2[0])}, s1 = {lo_f(xa2[1]), hi_f(xa2[1])}, t0 = {0.f, 0.f}, t1 = {0.f, 0.f};
#pragma unroll
      for (int cc = 0; cc < NCH; ++cc) { t0 += f32x2{lo_f(v2[cc][0]), hi_f(v2[cc][0])}; t1 += f32x2{lo_f(v2[cc][1]), hi_f(v2[cc][1])}; }
      *(u32x2*)(out + off) = u32x2{cvtpk(s0[0] + t0[0], s0[1] + t0[1]), cvtpk(s1[0] + t1[0], s1[1] + t1[1])};
    } else { WAITV(v); finish1(wid, lane, v, xa, xb); }
  }
  STAMP(10);
  if (blockIdx.x == 0 && wid == 0) {
#pragma unroll 1
    for (int i = 0; i < 4; ++i) fl_wait(FL(FL_B + i * 64 + lane), tag);
    if (lane == 0) fl_store(FL(0), tag);
  }
}

extern "C" void kernel_launch(void* const* d_in, const int* in_sizes, int n_in, void* d_out, int out_size, void* d_ws, size_t ws_size, hipStream_t stream) {
  const unsigned short* x = (const unsigned short*)d_in[0];
  const char* wqkv = (const char*)d_in[1];
  const char* wo = (const char*)d_in[2];
  const char* wg = (const char*)d_in[4]; const char* wu = (const char*)d_in[5]; const char* wd = (const char*)d_in[6];
  char* ws = (char*)d_ws;
  u64* fl64 = (u64*)ws;
  char* halo = ws + (1u << 20);
  char* xf = ws + (4u << 20);
  unsigned short* part = (unsigned short*)(ws + (8u << 20));
  char* wqp = ws + (48u << 20);
  (void)hipFuncSetAttribute((const void*)k_main, hipFuncAttributeMaxDynamicSharedMemorySize, LDS_BYTES);
  k_main<<<256, 512, LDS_BYTES, stream>>>(x, wqkv, wg, wu, wd, wo, part, halo, fl64, xf, wqp, (unsigned short*)d_out);
}
```

```cpp
#include <hip/hip_runtime.h>
#include <hip/hip_bf16.h>
#include <stdint.h>
#include <utility>
#include <type_traits>

using bf16x8 = __attribute__((ext_vector_type(8))) short;
using s16x4  = __attribute__((ext_vector_type(4))) short;
using f32x16 = __attribute__((ext_vector_type(16))) float;
using f32x2  = __attribute__((ext_vector_type(2))) float;
using u32x4  = __attribute__((ext_vector_type(4))) unsigned;
using u32x2  = __attribute__((ext_vector_type(2))) unsigned;
#define LAS __attribute__((address_space(3)))
typedef LAS const char* lds_cptr;
typedef LAS char* lds_ptr;
typedef short v4i16_t __attribute__((ext_vector_type(4)));
typedef __bf16 bf16x2_t __attribute__((ext_vector_type(2)));
#define DI __device__ __forceinline__
#define SBAR() __builtin_amdgcn_sched_barrier(0)

constexpr int S_ = 2048, D_ = 576, F_ = 4608;
constexpr int NKS = 36;
constexpr int NCH = 16;
constexpr int SL = 26624;
constexpr int NSL = 6;
constexpr int LDS_BYTES = 163840;
constexpr unsigned CODE_PF_BYTES = 53 * 1024;
constexpr int QP = 232;

DI unsigned cvtpk(float lo, float hi) { f32x2 v = {lo, hi}; bf16x2_t b = __builtin_convertvector(v, bf16x2_t); return __builtin_bit_cast(unsigned, b); }
DI float rbf(float f) { return __uint_as_float(cvtpk(f, 0.f) << 16); }
DI float lo_f(unsigned p) { return __uint_as_float(p << 16); }
DI float hi_f(unsigned p) { return __uint_as_float(p & 0xffff0000u); }
DI float dot2(unsigned a, unsigned b, float c) { return __builtin_amdgcn_fdot2_f32_bf16(__builtin_bit_cast(bf16x2_t, a), __builtin_bit_cast(bf16x2_t, b), c, false); }
DI s16x4 vtr(lds_cptr p) { return __builtin_bit_cast(s16x4, __builtin_amdgcn_ds_read_tr16_b64_v4i16((LAS v4i16_t*)p)); }
DI bf16x8 trfrag(lds_cptr p, int o0, int o1) { s16x4 a = vtr(p + o0); SBAR(); s16x4 b = vtr(p + o1); return __builtin_shufflevector(a, b, 0, 1, 2, 3, 4, 5, 6, 7); }
DI void glds16s(unsigned voff, const void* sbase, unsigned lds_base) {
  asm volatile("s_mov_b32 m0, %2\n\ts_nop 0\n\tglobal_load_lds_dwordx4 %0, %1" :: "v"(voff), "s"(sbase), "s"(lds_base) : "memory", "m0");
}
#define PSTORE(p, v) asm volatile("global_store_dwordx4 %0, %1, off sc0 sc1\n\ts_nop 1" :: "v"(p), "v"(v) : "memory")
#define MFMA(a, b, c) __builtin_amdgcn_mfma_f32_32x32x16_bf16((a), (b), (c), 0, 0, 0)
template <int N> DI void wait_bar() { asm volatile("s_waitcnt vmcnt(%0)\n\ts_barrier" :: "i"(N) : "memory"); }
template <int N> DI void wait_bar_l() { asm volatile("s_waitcnt vmcnt(%0) lgkmcnt(0)\n\ts_barrier" :: "i"(N) : "memory"); }
DI void bar_l() { asm volatile("s_waitcnt lgkmcnt(0)\n\ts_barrier" ::: "memory"); }
template <int C> DI lds_cptr vaddc(unsigned v) { unsigned r; if (C == 0) r = v; else asm volatile("v_add_u32_e32 %0, %2, %1" : "=v"(r) : "v"(v), "i"(C)); return (lds_cptr)(size_t)r; }
DI float relu1(float x) { float r; asm("v_max_f32_e32 %0, 0, %1" : "=v"(r) : "v"(x)); return r; }
DI unsigned fresh_lane() { unsigned l; asm volatile("v_mbcnt_lo_u32_b32 %0, -1, 0\n\tv_mbcnt_hi_u32_b32 %0, -1, %0" : "=v"(l)); return l; }


typedef unsigned long long u64;
#define FL(i) (fl64 + (size_t)(i) * 16)
constexpr int FL_LN = 1, FL_HALO = 257, FL_A = 513, FL_B = 769;
DI u64 fl_load(const u64* p) { return __hip_atomic_load(p, __ATOMIC_RELAXED, __HIP_MEMORY_SCOPE_AGENT); }
DI void fl_store(u64* p, u64 v) { __hip_atomic_store(p, v, __ATOMIC_RELAXED, __HIP_MEMORY_SCOPE_AGENT); }
DI void fl_wait(const u64* p, u64 tag) { unsigned spins = 0; while (!__all(fl_load(p) == tag) && ++spins < (1u << 22)) __builtin_amdgcn_s_sleep(1); asm volatile("" ::: "memory"); }
#define ST128(p, v) asm volatile("global_store_dwordx4 %0, %1, off sc0 sc1\n\ts_nop 1" :: "v"(p), "v"(v) : "memory")
#define ST64(p, v) asm volatile("global_store_dwordx2 %0, %1, off sc0 sc1" :: "v"(p), "v"(v) : "memory")

template <class F, int... I> DI void for_each_int(F&& f, std::integer_sequence<int, I...>) { (f(std::integral_constant<int, I>{}), ...); }
constexpr int PFST = 4, NPF = 1;
struct Ptrs { const char* wg; const char* wu; const char* xm; const char* wq; };

DI void dma_gu(const char* base, unsigned pj, unsigned hh, unsigned ldsdst) {
  const unsigned g = fresh_lane() + 64 * pj, row = (g * 1821u) >> 16;
  glds16s(row * 8640u + g * 16u, base + (size_t)hh * (16 * F_ * 2), ldsdst + pj * 1024);
}
DI void dma_x(const Ptrs& P, unsigned t, unsigned hh, unsigned so) { glds16s(fresh_lane() * 16u, P.xm + (size_t)(t * NKS + hh) * 1024, so + (18 + t) * 1024); }
DI void dma_q(const Ptrs& P, unsigned t, unsigned hh, unsigned so) { glds16s(fresh_lane() * 16u, P.wq + (size_t)hh * 4096 + t * 1024, so + (22 + t) * 1024); }
DI void dma_piece(const Ptrs& P, unsigned p, unsigned hh, unsigned so) {
  if (p < 9) dma_gu(P.wg, p, hh, so); else if (p < 18) dma_gu(P.wu, p - 9, hh, so + 9216); else if (p < 22) dma_x(P, p - 18, hh, so); else dma_q(P, p - 22, hh, so);
}

template <int FH, class F, class G> DI void phase1(F&& tail_dma, G&& l2_prefetch, const Ptrs& P, const unsigned w4, const unsigned l0, lds_cptr ldsb, const unsigned rdA, const unsigned rdX, const unsigned (&rdQ)[3], f32x16 (&acc)[11]) {
  constexpr int LA = 5, RING = 6;
  auto issue_common = [&](unsigned hh, unsigned so) {
    dma_gu(P.wg, w4, hh, so); dma_gu(P.wg, 4 + w4, hh, so); dma_gu(P.wu, w4, hh, so + 9216); dma_gu(P.wu, 4 + w4, hh, so + 9216); dma_x(P, w4, hh, so);
  };
  auto issue_evenB = [&](unsigned hh, unsigned so) {
    so += l0;
    dma_q(P, w4, hh, so);
    if (w4 < 2) dma_gu(w4 ? P.wu : P.wg, 8, hh, so + (w4 ? 9216 : 0));
  };
  auto issue_odd = [&](unsigned hh, unsigned so) { issue_common(hh, so + l0); issue_evenB(hh, so); };
  auto issue_evenA = [&](unsigned hh, unsigned so) { issue_common(hh, so + l0); };
  bf16x8 fr[RING]; bf16x8 xq[2];
  constexpr int NGU = FH ? 8 : 10;
  lds_cptr aG = nullptr, aQ[3] = {nullptr, nullptr, nullptr};
  const unsigned rA = l0 + rdA, rX = l0 + rdX, rQ0 = l0 + rdQ[0], rQ1 = l0 + rdQ[1], rQ2 = l0 + rdQ[2];
#define SET_SLOT(slot) do { aG = vaddc<(slot) * SL>(rA); aQ[0] = vaddc<(slot) * SL>(rQ0); if (FH) { aQ[1] = vaddc<(slot) * SL>(rQ1); aQ[2] = vaddc<(slot) * SL>(rQ2); } } while (0)
#define RD(dst, n) do { constexpr int nn_ = (n) % 66, slot_ = nn_ / 11, t_ = nn_ % 11; if (t_ == 0) SET_SLOT(slot_); \
    if (t_ < NGU) dst = trfrag(aG, (t_ & 1) * 9216 + (t_ >> 1) * 64, (t_ & 1) * 9216 + (t_ >> 1) * 64 + 4 * 576); else dst = trfrag(aQ[t_ < NGU ? 0 : t_ - NGU], 0, 4 * 256); } while (0)
#define RDX(slot) (*(const LAS bf16x8*)vaddc<(slot) * SL>(rX))
  xq[0] = RDX(0);
  SBAR();
  RD(fr[0], 0); SBAR(); RD(fr[1], 1); SBAR(); RD(fr[2], 2); SBAR(); RD(fr[3], 3); SBAR(); RD(fr[4], 4); SBAR();
#pragma unroll 1
  for (int it = 0; it < 6; ++it) {
    auto step = [&](auto n_) {
      constexpr int n = decltype(n_)::value;
      constexpr int S3 = n / 22, m = n % 22;
      const int st = 3 * it + S3;
      if (m == 22 - LA) {
        if (!FH && st <= 15) issue_evenA(2 * st + 4, ((2 * S3 + 4) % 6) * SL);
        if (st < 17) {
          if (st == 16) wait_bar<0>();
          else if (FH) { if (st == PFST + 1) { if (w4 < 2) wait_bar<2 + NPF>(); else wait_bar<1 + NPF>(); } else { if (w4 < 2) wait_bar<2>(); else wait_bar<1>(); } }
          else wait_bar<5>();
        }
        xq[0] = RDX((2 * S3 + 2) % 6);
        if (FH) { if (st <= 15) issue_odd(2 * st + 5, ((2 * S3 + 5) % 6) * SL); if (st <= 14) issue_evenB(2 * st + 6, ((2 * S3 + 6) % 6) * SL); }
        if (S3 == 1 && st == 16) tail_dma();
        if (FH && st == PFST) l2_prefetch();
      }
      if (m == 11 - LA) xq[1] = RDX(2 * S3 + 1);
      const bf16x8 xcur = xq[m / 11];
      RD(fr[(n + LA) % RING], n + LA);
      acc[m % 11] = MFMA(fr[n % RING], xcur, acc[m % 11]);
      SBAR();
    };
    for_each_int(step, std::make_integer_sequence<int, 66>{});
  }
#undef RD
#undef RDX
#undef SET_SLOT
}

__global__ __launch_bounds__(512, 2) void k_main(const unsigned short* __restrict__ x, const char* __restrict__ wqkv, const char* __restrict__ wg, const char* __restrict__ wu,
                                                 const char* __restrict__ wd, const char* __restrict__ wo, unsigned short* __restrict__ part,
                                                 char* __restrict__ halo, u64* __restrict__ fl64, char* __restrict__ xf, char* __restrict__ wqp,
                                                 unsigned short* __restrict__ out) {
  extern __shared__ __attribute__((aligned(16))) char lds[];
  const int tid = threadIdx.x, lane = tid & 63;
  const unsigned wid = __builtin_amdgcn_readfirstlane(tid >> 6);
  const unsigned tg = wid & 3, fh = wid >> 2;
  const unsigned bx = blockIdx.x, xcd = bx & 7, kk = bx >> 3;
  const unsigned c = xcd * 2 + (kk & 1), mt = kk >> 1;
  const unsigned l0 = (unsigned)(uintptr_t)lds;
  const lds_cptr ldsb = (lds_cptr)lds;
  Ptrs P;
  P.wg = wg + 576 * c; P.wu = wu + 576 * c; P.xm = xf + (size_t)(mt * 4) * NKS * 1024; P.wq = wqp + (size_t)c * (576 * 256);
  const unsigned h = lane >> 5, blk16 = (lane >> 4) & 1, q4 = (lane & 15) >> 2, p4 = lane & 3;
  const unsigned rdA = (8 * h + q4) * 576 + blk16 * 32 + p4 * 8 + fh * 5 * 64;
  const unsigned rdX = 18432 + tg * 1024 + lane * 16;
  unsigned rdQ[3];
#pragma unroll
  for (int i = 0; i < 3; ++i) { const unsigned t = fh ? i + 1 : 0; rdQ[i] = 22528 + (8 * h + q4) * 256 + ((t ^ q4) * 64) + blk16 * 32 + p4 * 8; }
  const unsigned lrow = lane >> 3, ch2 = (lane & 7) ^ (((lrow >> 1) & 1) << 2);
  const unsigned voff2 = lrow * (D_ * 2) + ch2 * 16;
  auto dma2j = [&](int q, unsigned j) {
    const int hh = (2 * q) % 6;
    const unsigned so = j >= 22 ? ((hh + 1) % 6) * SL + (j - 22) * 1024 : hh * SL + j * 1024;
    if (j < 36) glds16s(voff2, wd + (size_t)(288 * c + 8 * j) * (D_ * 2) + q * 128, l0 + so);
    else { const unsigned l = fresh_lane(), lr = l >> 3; unsigned wr = 36 * c + 8 * (j - 36) + lr; if (wr > 575) wr = 575;
      glds16s(wr * (D_ * 2) + (((l & 7) ^ (((lr >> 1) & 1) << 2)) * 16), wo + q * 128, l0 + so); }
  };
  auto dma2_A = [&](int q) {
#pragma unroll
    for (int k = 0; k < 5; ++k) dma2j(q, tg + 4 * k);
  };
  auto dma2_Bodd = [&](int q) {
#pragma unroll
    for (int k = 0; k < 5; ++k) dma2j(q, 22 + tg + 4 * k);
  };
  auto dma2_Beven = [&](int q) { if (tg < 2) dma2j(q, 20 + tg); };
  auto dma2_all = [&](int q) { if (fh) { dma2_Beven(q); dma2_Bodd(q); } else dma2_A(q); };
  auto l2_prefetch = [&]() {
    unsigned idx = tg * 64 + fresh_lane();
    const char* a;
    if (idx < 162) a = wd + (size_t)(288 * c + 18 * mt) * (D_ * 2) + idx * 128;
    else { unsigned k = idx - 162; if (k > 20) k = 20; k += mt * 21; if (k > 323) k = 323; a = wo + (size_t)(36 * c) * (D_ * 2) + k * 128; }
    asm volatile("s_mov_b32 m0, %1\n\ts_nop 0\n\tglobal_load_lds_dword %0, off" :: "v"(a), "s"(l0 + NSL * SL + wid * 256) : "memory", "m0");
  };
  if (wid == 4) {
    u64 pc; asm volatile("s_getpc_b64 %0" : "=s"(pc));
    unsigned off = (kk * 14 + (lane < 14 ? lane : 13)) * 128; if (off > CODE_PF_BYTES) off = CODE_PF_BYTES;
    asm volatile("s_mov_b32 m0, %1\n\ts_nop 0\n\tglobal_load_lds_dword %0, off" :: "v"((const char*)(pc & ~127ull) + off), "s"(l0 + NSL * SL + wid * 256) : "memory", "m0");
  }
  const u64 epoch = fl_load(FL(0));
  if (fh) {
#pragma unroll 1
    for (int hh = 0; hh < 4; ++hh)
#pragma unroll
      for (int k = 0; k < 5; ++k) if (tg + 4 * k < 18) dma_piece(P, tg + 4 * k, hh, l0 + hh * SL);
  } else if (wid == 0) {
    const int j = lane & 7, t = 128 * mt + 8 * c + (lane >> 3);
    const u32x4* src = (const u32x4*)(x + (size_t)t * D_) + j;
    u32x4 v[9];
#pragma unroll
    for (int i = 0; i < 9; ++i) v[i] = src[8 * i];
    float s = 0, q = 0;
#pragma unroll
    for (int i = 0; i < 9; ++i)
#pragma unroll
      for (int e = 0; e < 4; ++e) { s = dot2(v[i][e], 0x3f803f80u, s); q = dot2(v[i][e], v[i][e], q); }
#pragma unroll
    for (int o = 1; o < 8; o <<= 1) { s += __shfl_xor(s, o); q += __shfl_xor(q, o); }
    const float m = s * (1.f / D_);
    const float var = fmaxf(q * (1.f / D_) - m * m, 0.f);
    const float mb = rbf(m), vb = rbf(var);
    const float ri = 1.f / rbf(sqrtf(rbf(vb + 1e-5f)));
    const f32x2 mb2 = {mb, mb}, ri2 = {ri, ri};
    char* gdst = xf + (size_t)((t >> 5) * NKS) * 1024 + (t & 31) * 16 + (j & 1) * 512 + (j >> 1) * 1024;
#pragma unroll
    for (int i = 0; i < 9; ++i) {
      u32x4 o;
#pragma unroll
      for (int e = 0; e < 4; ++e) { f32x2 a = {lo_f(v[i][e]), hi_f(v[i][e])}; a -= mb2; const unsigned d = cvtpk(a[0], a[1]); f32x2 b = {lo_f(d), hi_f(d)}; b *= ri2; o[e] = cvtpk(b[0], b[1]); }
      ST128(gdst + i * 4096, o);
    }
    asm volatile("s_waitcnt vmcnt(0)" ::: "memory");
  } else {
    u32x2 w[6];
#pragma unroll
    for (int i = 0; i < 6; ++i) {
      const unsigned id = ((wid - 1) * 6 + i) * 64 + lane, row = 36 * mt + (id >> 5), qd = id & 31, pt = qd / 9;
      w[i] = u32x2{0u, 0u};
      if (qd < 27) w[i] = *(const u32x2*)(wqkv + (size_t)row * 3456 + pt * 1152 + 72 * c + (qd - 9 * pt) * 8);
    }
#pragma unroll
    for (int i = 0; i < 6; ++i) {
      const unsigned id = ((wid - 1) * 6 + i) * 64 + lane, row = 36 * mt + (id >> 5), qd = id & 31;
      ST64(wqp + (size_t)(c * 576 + row) * 256 + (((qd >> 3) ^ (row & 3)) * 64) + (qd & 7) * 8, w[i]);
    }
    asm volatile("s_waitcnt vmcnt(0)" ::: "memory");
  }
  const u64 tag = (((u64)(unsigned)__builtin_amdgcn_readfirstlane((unsigned)(epoch >> 32)) << 32) | (u64)(unsigned)__builtin_amdgcn_readfirstlane((unsigned)epoch)) + 1ull;
  __builtin_amdgcn_s_barrier();
  if (wid == 0) {
    if (lane == 0) fl_store(FL(FL_LN + mt * 16 + c), tag);
    fl_wait(FL(FL_LN + ((lane & 16) ? (lane & 15) * 16 + c : mt * 16 + (lane & 15))), tag);
  }
  __builtin_amdgcn_s_barrier();
#pragma unroll
  for (int k = 0; k < 5; ++k) { const unsigned i = wid + 8 * k;
    if (i < 32) dma_piece(P, 18 + (i & 7), i >> 3, l0 + (i >> 3) * SL);
    else if (i < 38) dma_piece(P, i < 36 ? 22 + (i - 32) : (i == 36 ? 8 : 17), 4, l0 + 4 * SL); }
  wait_bar<0>();
  const f32x16 zero = {};
  f32x16 acc[11];
#pragma unroll
  for (int i = 0; i < 11; ++i) acc[i] = zero;
  if (fh) phase1<1>([&]() { dma2_all(0); }, l2_prefetch, P, tg, l0, ldsb, rdA, rdX, rdQ, acc); else phase1<0>([&]() { dma2_all(0); }, l2_prefetch, P, tg, l0, ldsb, rdA, rdX, rdQ, acc);
  const unsigned tag_lo = (unsigned)tag, tag_hi = (unsigned)(tag >> 32);
  if (wid == 7) {
    char* hb = halo + (size_t)(mt * 16 + c) * 8192 + ((lane & 15) * 288);
    if (lane & 16) {
#pragma unroll
      for (int t = 1; t < 4; ++t)
#pragma unroll
        for (int g = 0; g < 4; ++g) {
          const int c0 = 32 * t + 8 * g;
          if (c0 + 4 >= 36 && c0 < 108) {
            const u32x4 w = {cvtpk(acc[7 + t][4 * g], acc[7 + t][4 * g + 1]), cvtpk(acc[7 + t][4 * g + 2], acc[7 + t][4 * g + 3]), tag_lo, tag_hi};
            const int cc = c0 + 4 * (int)h;
            if (cc >= 36 && cc < 108) asm volatile("global_store_dwordx4 %0, %1, off sc0 sc1\n\ts_nop 1" :: "v"(hb + (cc - 36) * 4), "v"(w) : "memory");
          }
        }
    }
  }
  u32x4 own[10];
#pragma unroll
  for (int b = 0; b < 5; ++b) if (b < 4 || fh == 0)
#pragma unroll
    for (int i = 0; i < 8; ++i) {
      f32x2 g2 = {relu1(acc[2 * b][2 * i]), relu1(acc[2 * b][2 * i + 1])};
      g2 *= f32x2{acc[2 * b + 1][2 * i], acc[2 * b + 1][2 * i + 1]};
      own[2 * b + (i >> 2)][i & 3] = cvtpk(g2[0], g2[1]);
    }
  bar_l();
  constexpr int HX = 2 * SL, QK = HX + 73728;
  const unsigned hxo = HX + tg * 18432 + lane * 16;
  {
    const lds_ptr hx = (lds_ptr)lds + hxo + fh * 10240;
#pragma unroll
    for (int i = 0; i < 10; ++i) if (i < 8 || fh == 0) *(LAS u32x4*)(hx + i * 1024) = own[i];
    lds_ptr d = (lds_ptr)lds + QK + (16 + tg * 32 + (lane & 31)) * QP + 8 * h;
    auto put = [&](const f32x16& a, int t) {
#pragma unroll
      for (int g = 0; g < 4; ++g) if (32 * t + 8 * g < 108) {
        const u32x2 w = {cvtpk(a[4 * g], a[4 * g + 1]), cvtpk(a[4 * g + 2], a[4 * g + 3])};
        if (32 * t + 8 * g + 4 < 108 || h == 0) *(LAS u32x2*)(d + (32 * t + 8 * g) * 2) = w;
      }
    };
    if (fh) { put(acc[8], 1); put(acc[9], 2); put(acc[10], 3); } else put(acc[10], 0);
  }
  if (mt > 0 && wid == 0) {
    const char* hb = halo + (size_t)((mt - 1) * 16 + c) * 8192;
    u32x4 v[5]; unsigned spins = 0;
    for (;;) {
#pragma unroll
      for (int i = 0; i < 5; ++i) { unsigned id = lane + 64 * i; if (id > 287) id = 287; asm volatile("global_load_dwordx4 %0, %1, off sc0 sc1" : "=v"(v[i]) : "v"(hb + id * 16) : "memory"); }
      asm volatile("s_waitcnt vmcnt(0)" : "+v"(v[0]), "+v"(v[1]), "+v"(v[2]), "+v"(v[3]), "+v"(v[4]) :: "memory");
      bool ok = true;
#pragma unroll
      for (int i = 0; i < 5; ++i) ok = ok && v[i][2] == tag_lo && v[i][3] == tag_hi;
      if (__all(ok) || ++spins >= (1u << 20)) break;
      __builtin_amdgcn_s_sleep(1);
    }
#pragma unroll
    for (int i = 0; i < 5; ++i) {
      const unsigned id = lane + 64 * i;
      if (id < 288) { const unsigned tk = id / 18, ck = id - tk * 18; *(LAS u32x2*)((lds_ptr)lds + QK + tk * QP + 72 + ck * 8) = u32x2{v[i][0], v[i][1]}; }
    }
  }
  if (mt == 0 && wid == 0) {
#pragma unroll
    for (int i = 0; i < 8; ++i) { const unsigned id = lane + 64 * i; if (id < 16 * (QP / 8)) *(LAS u32x2*)((lds_ptr)lds + QK + id * 8) = u32x2{0u, 0u}; }
  }
  bar_l();
  bf16x8 hf[21];
#pragma unroll
  for (int s = 0; s < 18; ++s) hf[s] = *(const LAS bf16x8*)(ldsb + hxo + s * 1024);
  float r10[10];
  const int tok = (tid & 31) + 32 * ((tid >> 6) & 3), wp = (tid >> 5) & 1; const int head = tid >> 8;
  {
    const int t = 128 * mt + tok, slot = 16 + tok;
    const int ox = head * 40, oy = head ? 36 : 32;
    const lds_cptr qp = ldsb + QK + slot * QP;
    unsigned qv[9];
#pragma unroll
    for (int i = 0; i < 4; ++i) { const u32x2 u = *(const LAS u32x2*)(qp + ox + 8 * i); qv[2 * i] = u[0]; qv[2 * i + 1] = u[1]; }
    qv[8] = *(const LAS unsigned*)(qp + oy);
    float lg[4];
#pragma unroll
    for (int i4 = 0; i4 < 4; ++i4) {
      const int w = 4 * wp + i4;
      const lds_cptr kp = qp - 2 * w * QP + 72;
      float a = 0;
#pragma unroll
      for (int i = 0; i < 4; ++i) { const u32x2 u = *(const LAS u32x2*)(kp + ox + 8 * i); a = dot2(qv[2 * i], u[0], a); a = dot2(qv[2 * i + 1], u[1], a); }
      a = dot2(qv[8], *(const LAS unsigned*)(kp + oy), a);
      lg[i4] = (t - 2 * w >= 0) ? rbf(rbf(a) * 0.23570226039551584f) : -1e9f;
    }
    float mx = fmaxf(fmaxf(lg[0], lg[1]), fmaxf(lg[2], lg[3]));
    mx = fmaxf(mx, __shfl_xor(mx, 32));
    float sum = 0;
#pragma unroll
    for (int i = 0; i < 4; ++i) { lg[i] = __expf(lg[i] - mx); sum += lg[i]; }
    sum += __shfl_xor(sum, 32);
    const float inv = 1.f / sum;
    f32x2 o[9];
#pragma unroll
    for (int i = 0; i < 9; ++i) o[i] = f32x2{0.f, 0.f};
#pragma unroll
    for (int i4 = 0; i4 < 4; ++i4) {
      const int w = 4 * wp + i4;
      const float p = rbf(lg[i4] * inv); const f32x2 p2 = {p, p};
      const lds_cptr vp = qp - 2 * w * QP + 144;
#pragma unroll
      for (int i = 0; i < 4; ++i) { const u32x2 u = *(const LAS u32x2*)(vp + ox + 8 * i);
        o[2 * i] += p2 * f32x2{lo_f(u[0]), hi_f(u[0])}; o[2 * i + 1] += p2 * f32x2{lo_f(u[1]), hi_f(u[1])}; }
      const unsigned y = *(const LAS unsigned*)(vp + oy);
      o[8] += p2 * f32x2{lo_f(y), hi_f(y)};
    }
#pragma unroll
    for (int i = 0; i < 5; ++i) {
      f32x2 a = (i < 4) ? o[i] : o[8], b = (i < 4) ? o[4 + i] : f32x2{0.f, 0.f};
      asm volatile("" : "+v"(a), "+v"(b));
      const f32x2 send = wp ? a : b, keep = wp ? b : a;
      r10[2 * i] = keep[0] + __shfl_xor(send[0], 32); r10[2 * i + 1] = keep[1] + __shfl_xor(send[1], 32);
    }
  }
  bar_l();
  {
    lds_ptr op = (lds_ptr)lds + HX + tok * 104 + head * 36;
#pragma unroll
    for (int i = 0; i < 4; ++i) *(LAS unsigned*)(op + 4 * (head + 4 * wp + i)) = cvtpk(r10[2 * i], r10[2 * i + 1]);
    if (wp == 0) *(LAS unsigned*)(op + (head ? 0 : 32)) = cvtpk(r10[8], r10[9]);
    if (head == 1 && wp == 1) { lds_ptr z = (lds_ptr)lds + HX + tok * 104 + 72; *(LAS u32x2*)z = u32x2{0u, 0u}; *(LAS u32x2*)(z + 8) = u32x2{0u, 0u}; *(LAS u32x2*)(z + 16) = u32x2{0u, 0u}; }
  }
  bar_l();
#pragma unroll
  for (int s = 0; s < 3; ++s) {
    const lds_cptr p = ldsb + HX + (tg * 32 + (lane & 31)) * 104 + (16 * s + 4 * h) * 2;
    const u32x2 a = *(const LAS u32x2*)p, b = *(const LAS u32x2*)(p + 16);
    hf[18 + s] = __builtin_bit_cast(bf16x8, u32x4{a[0], a[1], b[0], b[1]});
  }
  wait_bar_l<0>();
  dma2_all(1); dma2_all(2);
  const unsigned colb = (((4 * fh + 2 * blk16 + (p4 >> 1)) ^ ((q4 >> 1) << 2)) * 16) + 8 * (p4 & 1);
  const lds_cptr rb2 = ldsb + (4 * h + q4) * 128 + colb;
  unsigned short* pp = part + (((size_t)(mt * 4 + tg) * 18 + fh) * 32 + c) * 512 + lane * 8;
  {
    constexpr int LA = 6, RING = 7, CNT = 21;
    bf16x8 fr[RING];
    auto rd2 = [&](int q, int n) -> bf16x8 {
      const int hh = (2 * q + (n >= 11 ? 1 : 0)) % 6, nn = n >= 11 ? n - 11 : n;
      return trfrag(rb2 + hh * SL, nn * 2048, nn * 2048 + 1024);
    };
#pragma unroll
    for (int n = 0; n < LA; ++n) { fr[n] = rd2(0, n); SBAR(); }
    f32x16 acc2[2];
#pragma unroll
    for (int q = 0; q < 9; ++q) {
#pragma unroll
      for (int n = 0; n < CNT; ++n) {
        if (n == CNT - LA && q < 8) {
          if (!fh && q >= 1 && q + 2 < 9) dma2_A(q + 2);
          if (fh) {
            if (q == 0) { if (tg < 2) wait_bar<6>(); else wait_bar<5>(); }
            else if (q < 7) { if (tg < 2) wait_bar<3>(); else wait_bar<2>(); }
            else wait_bar<2>();
          } else {
            if (q == 0) wait_bar<5>(); else if (q < 7) wait_bar<7>(); else wait_bar<2>();
          }
          if (fh) { if (q >= 1 && q + 2 < 9) dma2_Bodd(q + 2); if (q + 3 < 9) dma2_Beven(q + 3); }
          if (q == 7 && tid == 64) fl_store(FL(FL_A + mt * 16 + c), tag);
        }
        if (n + LA < CNT) fr[(n + LA) % RING] = rd2(q, n + LA);
        else if (q < 8) fr[(n + LA) % RING] = rd2(q + 1, n + LA - CNT);
        acc2[q & 1] = MFMA(fr[n % RING], hf[n], n == 0 ? zero : acc2[q & 1]);
        if (n == 3 && q > 0) {
          u32x4 o0, o1;
#pragma unroll
          for (int i = 0; i < 4; ++i) { o0[i] = cvtpk(acc2[(q - 1) & 1][2 * i], acc2[(q - 1) & 1][2 * i + 1]); o1[i] = cvtpk(acc2[(q - 1) & 1][8 + 2 * i], acc2[(q - 1) & 1][8 + 2 * i + 1]); }
          PSTORE(pp + (q - 1) * 32768, o0); PSTORE(pp + (q - 1) * 32768 + 8192, o1);
        }
        SBAR();
      }
    }
    u32x4 o0, o1;
#pragma unroll
    for (int i = 0; i < 4; ++i) { o0[i] = cvtpk(acc2[0][2 * i], acc2[0][2 * i + 1]); o1[i] = cvtpk(acc2[0][8 + 2 * i], acc2[0][8 + 2 * i + 1]); }
    PSTORE(pp + 8 * 32768, o0); PSTORE(pp + 8 * 32768 + 8192, o1);
  }
  asm volatile("s_waitcnt vmcnt(0)" ::: "memory");
  __builtin_amdgcn_s_barrier();
  if (tid == 0) fl_store(FL(FL_B + mt * 16 + c), tag);
  {
    fl_wait(FL((wid < 6 ? FL_A : FL_B) + mt * 16 + (lane & 15)), tag);
  }
  {
    const unsigned wt = c >> 1, tb = mt * 4 + (wt & 3), hf2 = c & 1;
    auto reduce1 = [&](unsigned r, unsigned ln, u32x4 (&v)[NCH], u32x2& xa, u32x2& xb) {
      const unsigned db = 2 * r + (wt >> 2);
      const unsigned short* src = part + ((size_t)(tb * 18 + db) * 2 + hf2) * 8192 + ln * 8;
#pragma unroll
      for (int cc = 0; cc < NCH; ++cc) asm volatile("global_load_dwordx4 %0, %1, off sc0 sc1" : "=v"(v[cc]) : "v"(src + cc * 512) : "memory");
      const size_t off = (size_t)(tb * 32 + (ln & 31)) * D_ + db * 32 + hf2 * 16 + 4 * (ln >> 5);
      xa = *(const u32x2*)(x + off); xb = *(const u32x2*)(x + off + 8);
    };
#define WAITV(v) asm volatile("s_waitcnt vmcnt(0)" : "+v"(v[0]), "+v"(v[1]), "+v"(v[2]), "+v"(v[3]), "+v"(v[4]), "+v"(v[5]), "+v"(v[6]), "+v"(v[7]), "+v"(v[8]), "+v"(v[9]), "+v"(v[10]), "+v"(v[11]), "+v"(v[12]), "+v"(v[13]), "+v"(v[14]), "+v"(v[15]) :: "memory")
    auto finish1 = [&](unsigned r, unsigned ln, const u32x4 (&v)[NCH], const u32x2& xa, const u32x2& xb) {
      const unsigned db = 2 * r + (wt >> 2);
      const size_t off = (size_t)(tb * 32 + (ln & 31)) * D_ + db * 32 + hf2 * 16 + 4 * (ln >> 5);
      f32x2 a[4] = {{lo_f(xa[0]), hi_f(xa[0])}, {lo_f(xa[1]), hi_f(xa[1])}, {lo_f(xb[0]), hi_f(xb[0])}, {lo_f(xb[1]), hi_f(xb[1])}};
      f32x2 sm[4] = {{0.f, 0.f}, {0.f, 0.f}, {0.f, 0.f}, {0.f, 0.f}};
#pragma unroll
      for (int cc = 0; cc < NCH; ++cc) {
#pragma unroll
        for (int i = 0; i < 4; ++i) sm[i] += f32x2{lo_f(v[cc][i]), hi_f(v[cc][i])};
        if ((cc & 3) == 3) SBAR(); }
      u32x2 o0 = {cvtpk(a[0][0] + sm[0][0], a[0][1] + sm[0][1]), cvtpk(a[1][0] + sm[1][0], a[1][1] + sm[1][1])};
      u32x2 o1 = {cvtpk(a[2][0] + sm[2][0], a[2][1] + sm[2][1]), cvtpk(a[3][0] + sm[3][0], a[3][1] + sm[3][1])};
      *(u32x2*)(out + off) = o0; *(u32x2*)(out + off + 8) = o1;
    };
    u32x4 v[NCH]; u32x2 xa, xb;
    reduce1(wid, lane, v, xa, xb);
    if (wid >= 6) {
      const unsigned u = (wid - 6) * 64 + lane, ln = u >> 1, g = u & 1, db = 16 + (wt >> 2);
      const unsigned short* src = part + ((size_t)(tb * 18 + db) * 2 + hf2) * 8192 + ln * 8 + g * 4;
      u32x2 v2[NCH];
#pragma unroll
      for (int cc = 0; cc < NCH; ++cc) asm volatile("global_load_dwordx2 %0, %1, off sc0 sc1" : "=v"(v2[cc]) : "v"(src + cc * 512) : "memory");
      const size_t off = (size_t)(tb * 32 + (ln & 31)) * D_ + db * 32 + hf2 * 16 + 4 * (ln >> 5) + 8 * g;
      const u32x2 xa2 = *(const u32x2*)(x + off);
      WAITV(v); WAITV(v2);
      SBAR();
      finish1(wid, lane, v, xa, xb);
      SBAR();
      f32x2 s0 = {lo_f(xa2[0]), hi_f(xa2[0])}, s1 = {lo_f(xa2[1]), hi_f(xa2[1])}, t0 = {0.f, 0.f}, t1 = {0.f, 0.f};
#pragma unroll
      for (int cc = 0; cc < NCH; ++cc) { t0 += f32x2{lo_f(v2[cc][0]), hi_f(v2[cc][0])}; t1 += f32x2{lo_f(v2[cc][1]), hi_f(v2[cc][1])}; }
      *(u32x2*)(out + off) = u32x2{cvtpk(s0[0] + t0[0], s0[1] + t0[1]), cvtpk(s1[0] + t1[0], s1[1] + t1[1])};
    } else { WAITV(v); finish1(wid, lane, v, xa, xb); }
  }
  if (blockIdx.x == 0 && wid == 0) {
#pragma unroll 1
    for (int i = 0; i < 4; ++i) fl_wait(FL(FL_B + i * 64 + lane), tag);
    if (lane == 0) fl_store(FL(0), tag);
  }
}

extern "C" void kernel_launch(void* const* d_in, const int* in_sizes, int n_in, void* d_out, int out_size, void* d_ws, size_t ws_size, hipStream_t stream) {
  const unsigned short* x = (const unsigned short*)d_in[0];
  const char* wqkv = (const char*)d_in[1];
  const char* wo = (const char*)d_in[2];
  const char* wg = (const char*)d_in[4]; const char* wu = (const char*)d_in[5]; const char* wd = (const char*)d_in[6];
  char* ws = (char*)d_ws;
  u64* fl64 = (u64*)ws;
  char* halo = ws + (1u << 20);
  char* xf = ws + (4u << 20);
  unsigned short* part = (unsigned short*)(ws + (8u << 20));
  char* wqp = ws + (48u << 20);
  (void)hipFuncSetAttribute((const void*)k_main, hipFuncAttributeMaxDynamicSharedMemorySize, LDS_BYTES);
  k_main<<<256, 512, LDS_BYTES, stream>>>(x, wqkv, wg, wu, wd, wo, part, halo, fl64, xf, wqp, (unsigned short*)d_out);
}
```
